```python
import torch

def _softmax(x, axis=-1):
    return torch.softmax(x, dim=axis)
def _gelu(x):
    return torch.nn.functional.gelu(x, approximate="tanh")
def _silu(x):
    return torch.nn.functional.silu(x)
def _sigmoid(x):
    return torch.sigmoid(x)
def _layernorm(x, axis=-1, eps=1e-5):
    m = x.mean(dim=axis, keepdim=True)
    v = x.var(dim=axis, keepdim=True, unbiased=False)
    return (x - m) / torch.sqrt(v + eps)
def _scatter_add(tgt, idx, src, axis=0):
    return tgt.clone().index_add_(axis, idx, src)
def _segment_sum(src, seg, n):
    out = torch.zeros((n,) + src.shape[1:], dtype=src.dtype)
    return out.index_add_(0, seg, src)
def _pad(x, pw):
    flat = tuple(v for pair in reversed(pw) for v in pair)
    return torch.nn.functional.pad(x, flat)

kernel_name = 'concat_heads__ssm_conv_h3__mha_block__prompt'

def setup_inputs():

    _w = (lambda a: (lambda t: t.to(torch.bfloat16) if t.is_floating_point() else t)(torch.as_tensor(a)))
    def _A_setup():
        B, S, D = 1, 2048, 1024
        ks = 4
        return {
            "q": _w((lambda *s: torch.randn(*s, dtype=torch.bfloat16) * 0.05)(B, S, D)),
            "k": _w((lambda *s: torch.randn(*s, dtype=torch.bfloat16) * 0.8)(B, S, D)),
            "v": _w((lambda *s: torch.randn(*s, dtype=torch.bfloat16) * 0.8)(B, S, D)),
            "shift_k": _w((lambda *s: torch.randn(*s, dtype=torch.bfloat16) * 0.8)(D, ks) * 0.5),
            "ssm_k": _w((lambda *s: torch.randn(*s, dtype=torch.bfloat16) * 0.8)(D, S) * (1.0 / S ** 0.5)),
            "ssm_D": _w((lambda *s: torch.randn(*s, dtype=torch.bfloat16) * 0.8)(D)),
        }
    _A_in = _A_setup()

    def _B_setup():
        B, S, D, H = 1, 2048, 1024, 32
        return {
            "x": _w((lambda *s: torch.randn(*s, dtype=torch.bfloat16) * 0.2)(B, S, D)),
            "wqkv": _w((lambda *s: torch.randn(*s, dtype=torch.bfloat16) * 0.2)(D, 3 * D)),
            "wo": _w((lambda *s: torch.randn(*s, dtype=torch.bfloat16) * 0.05)(D, D)),
        }
    _B_in = _B_setup()

    return {
        "A_q": _A_in["q"],
        "A_k": _A_in["k"],
        "A_v": _A_in["v"],
        "A_shift_k": _A_in["shift_k"],
        "A_ssm_k": _A_in["ssm_k"],
        "A_ssm_D": _A_in["ssm_D"],
        "B_x": _B_in["x"],
        "B_wqkv": _B_in["wqkv"],
        "B_wo": _B_in["wo"],
    }

def reference(A_q, A_k, A_v, A_shift_k, A_ssm_k, A_ssm_D, B_x, B_wqkv, B_wo):
    def _A_ref(q, k, v, shift_k, ssm_k, ssm_D):
        B, S, D = q.shape
        ks = shift_k.shape[-1]
        k_pad = _pad(k, ((0, 0), (ks - 1, 0), (0, 0)))
        k_shift = torch.zeros_like(k)
        for t in range(ks):
            k_shift = k_shift + k_pad[:, t:t + S, :] * shift_k[:, ks - 1 - t][None, None, :]
        kv = k_shift * v
        L = 2 * S
        kv_pad = _pad(kv, ((0, 0), (0, S), (0, 0)))
        kf_pad = _pad(ssm_k, ((0, 0), (0, S)))
        kv_f = (lambda x, axes=(-2,-1): torch.fft.rfft2(x.float(), dim=axes))(kv_pad[:, None, :, :], axes=(1, 2))
        kf_f = (lambda x, axes=(-2,-1): torch.fft.rfft2(x.float(), dim=axes))(kf_pad[:, None, :], axes=(1, 2))
        kf_f = (lambda x, *d: x.permute(*d))(kf_f, 1, 2, 0)
        y_f = kv_f * kf_f[None, :, :, :]
        y = (lambda x, s, axes=(-2,-1): torch.fft.irfft2(x, s=s, dim=axes).to(torch.bfloat16))(y_f, (1, L), axes=(1, 2))[:, 0, :S, :]
        y = y + kv * ssm_D[None, None, :]
        return q * y

    def _B_ref(x, wqkv, wo):
        B, S, D = x.shape
        H = 32
        Dh = D // H
        qkv = (lambda x, *d: x.permute(*d))((x @ wqkv).reshape(B, S, 3, H, Dh), 2, 0, 3, 1, 4)
        q, k, v = qkv[0], qkv[1], qkv[2]
        scores = torch.einsum("bhqd,bhkd->bhqk", q, k) * (Dh ** -0.5)
        p = _softmax(scores, axis=-1)
        o = torch.einsum("bhqk,bhkd->bhqd", p, v)
        return (lambda x, *d: x.permute(*d))(o, 0, 2, 1, 3).reshape(B, S, D) @ wo

    a = _A_ref(A_q, A_k, A_v, A_shift_k, A_ssm_k, A_ssm_D)
    b = _B_ref(B_x, B_wqkv, B_wo)
    return torch.concatenate([a, b], axis=-1)
```

```cpp
#include <hip/hip_runtime.h>
#include <hip/hip_bf16.h>
#include <stdint.h>

typedef short bf16x8 __attribute__((ext_vector_type(8)));
typedef short s16x4 __attribute__((ext_vector_type(4)));
typedef float f32x16 __attribute__((ext_vector_type(16)));
typedef float f32x4 __attribute__((ext_vector_type(4)));
typedef float f32x2 __attribute__((ext_vector_type(2)));
typedef __bf16 bf16x2n __attribute__((ext_vector_type(2)));
typedef unsigned u32x4 __attribute__((ext_vector_type(4)));
typedef unsigned u32x2 __attribute__((ext_vector_type(2)));
typedef short v4i16_t __attribute__((ext_vector_type(4)));
#define LAS __attribute__((address_space(3)))
typedef LAS char* lds_ptr;

#define SBAR() __builtin_amdgcn_sched_barrier(0)
#define MFMA32(a, b, c) __builtin_amdgcn_mfma_f32_32x32x16_bf16(a, b, c, 0, 0, 0)
#define MFMA16(a, b, c) __builtin_amdgcn_mfma_f32_16x16x32_bf16(a, b, c, 0, 0, 0)
#define WAIT_VM(n) asm volatile("s_waitcnt vmcnt(" #n ")" ::: "memory")
#define WAIT_LGKM0() asm volatile("s_waitcnt lgkmcnt(0)" ::: "memory")
#define RAW_BAR() __builtin_amdgcn_s_barrier()

constexpr int S_LEN = 2048, DM = 1024, NH = 32;
constexpr float QSCALE = 0.17677669529663687f * 1.4426950408889634f;

constexpr size_t WS_Q = 0;
constexpr size_t WS_KV = 4u << 20;
constexpr size_t WS_O = 12u << 20;

__device__ __forceinline__ unsigned cvtpk(float lo, float hi) {
    return __builtin_bit_cast(unsigned, __builtin_convertvector((f32x2){lo, hi}, bf16x2n));
}
__device__ __forceinline__ float bf_lo(unsigned w) { return __uint_as_float(w << 16); }
__device__ __forceinline__ float bf_hi(unsigned w) { return __uint_as_float(w & 0xffff0000u); }
__device__ __forceinline__ float rbf(float x) { return bf_lo(cvtpk(x, x)); }
__device__ __forceinline__ void glds16(const void* g, lds_ptr l) {
    __builtin_amdgcn_global_load_lds((const unsigned*)g, (LAS unsigned*)l, 16, 0, 0);
}
__device__ __forceinline__ bf16x8 lds_rd128(lds_ptr p) { return *(const LAS bf16x8*)p; }
__device__ __forceinline__ s16x4 lds_tr(lds_ptr p) {
    return __builtin_bit_cast(s16x4, __builtin_amdgcn_ds_read_tr16_b64_v4i16((LAS v4i16_t*)p));
}
__device__ __forceinline__ bf16x8 cat4(s16x4 a, s16x4 b) { return (bf16x8){a[0], a[1], a[2], a[3], b[0], b[1], b[2], b[3]}; }
__device__ __forceinline__ u32x4 pack8(const f32x16& v, int o, float sc) {
    return (u32x4){cvtpk(v[o] * sc, v[o + 1] * sc), cvtpk(v[o + 2] * sc, v[o + 3] * sc), cvtpk(v[o + 4] * sc, v[o + 5] * sc), cvtpk(v[o + 6] * sc, v[o + 7] * sc)};
}

constexpr int G1_STAGE = 40960, G1_WOFF = 16384, G1_LDS = 3 * G1_STAGE;

__global__ __launch_bounds__(512, 2) void k_qkv(const char* __restrict__ x, const char* __restrict__ w, char* __restrict__ ws) {
    extern __shared__ __attribute__((aligned(16))) char smem[];
    lds_ptr lds = (lds_ptr)smem;
    const int tid = threadIdx.x, lane = tid & 63;
    const int wid = __builtin_amdgcn_readfirstlane(tid >> 6);
    const int xcd = blockIdx.x & 7, idx = blockIdx.x >> 3;
    const int mt = 8 * (xcd & 1) + (idx & 7), p = 4 * (xcd >> 1) + (idx >> 3);
    if (wid >= 4) {
        const int pw = wid - 4;
        const char* xg0; const char* xg1; const char* wg;
        {
            const int rl = lane >> 3, c = lane & 7;
            xg0 = x + (size_t)(128 * mt + 32 * pw + rl) * 2048 + ((c ^ (lane >> 4)) << 4);
            xg1 = x + (size_t)(128 * mt + 32 * pw + rl) * 2048 + ((c ^ (4 + (lane >> 4))) << 4);
            const int sub = lane >> 5, row = (lane >> 2) & 7, cc = lane & 3;
            wg = w + (size_t)(16 * pw + row) * 6144 + p * 128 + sub * 64 + cc * 16;
        }
        auto issue = [&](int kt, int slot) {
            lds_ptr sb = lds + slot * G1_STAGE;
            const char* xa = xg0 + kt * 128; const char* xb = xg1 + kt * 128;
            glds16(xa, sb + (4 * pw + 0) * 1024);
            glds16(xb + 8 * 2048, sb + (4 * pw + 1) * 1024);
            glds16(xa + 16 * 2048, sb + (4 * pw + 2) * 1024);
            glds16(xb + 24 * 2048, sb + (4 * pw + 3) * 1024);
            const char* wk = wg + (size_t)kt * 64 * 6144;
#pragma unroll
            for (int kb = 0; kb < 2; ++kb)
#pragma unroll
                for (int seg = 0; seg < 3; ++seg)
                    glds16(wk + kb * 8 * 6144 + seg * 2048, sb + G1_WOFF + ((2 * pw + kb) * 3 + seg) * 1024);
        };
        issue(0, 0); issue(1, 1); issue(2, 2);
        WAIT_VM(20); RAW_BAR();
        int slot = 0;
        for (int j = 1; j < 16; ++j) {
            if (j + 1 < 16) { WAIT_VM(10); } else { WAIT_VM(0); }
            RAW_BAR();
            if (j + 2 < 16) issue(j + 2, slot);
            slot = (slot == 2) ? 0 : slot + 1;
        }
        return;
    }
    const int wm = wid >> 1, wn = wid & 1;
    const int r = lane & 31, hh = lane >> 5;
    int xa[4];
#pragma unroll
    for (int ks = 0; ks < 4; ++ks) xa[ks] = (64 * wm + r) * 128 + (((2 * ks + hh) ^ ((r >> 1) & 7)) << 4);
    const int wa = G1_WOFF + hh * 3072 + wn * 512 + ((lane >> 2) & 3) * 64 + ((lane >> 4) & 1) * 32 + (lane & 3) * 8;
    f32x16 acc[2][3];
#pragma unroll
    for (int i = 0; i < 2; ++i)
#pragma unroll
        for (int g = 0; g < 3; ++g) acc[i][g] = (f32x16){};
    bf16x8 xf[2][2], wf[2][3];
    auto ldfrag = [&](int buf, lds_ptr sb, int ks) {
        xf[buf][0] = lds_rd128(sb + xa[ks]);
        xf[buf][1] = lds_rd128(sb + xa[ks] + 4096);
#pragma unroll
        for (int g = 0; g < 3; ++g) {
            s16x4 lo = lds_tr(sb + wa + ks * 6144 + g * 1024);
            s16x4 hi = lds_tr(sb + wa + ks * 6144 + g * 1024 + 256);
            wf[buf][g] = cat4(lo, hi);
        }
    };
    auto mma = [&](int buf) {
#pragma unroll
        for (int tt = 0; tt < 2; ++tt) {
            acc[tt][0] = MFMA32(wf[buf][0], xf[buf][tt], acc[tt][0]);
            acc[tt][1] = MFMA32(wf[buf][1], xf[buf][tt], acc[tt][1]);
            acc[tt][2] = MFMA32(xf[buf][tt], wf[buf][2], acc[tt][2]);
        }
    };
    RAW_BAR();
    SBAR();
    ldfrag(0, lds, 0);
    int slot = 0;
    for (int kt = 0; kt < 16; ++kt) {
        lds_ptr sb = lds + slot * G1_STAGE;
        ldfrag(1, sb, 1); mma(0);
        ldfrag(0, sb, 2); mma(1);
        ldfrag(1, sb, 3); mma(0);
        slot = (slot == 2) ? 0 : slot + 1;
        if (kt + 1 < 16) {
            WAIT_LGKM0(); RAW_BAR(); SBAR();
            ldfrag(0, lds + slot * G1_STAGE, 0);
        }
        mma(1);
    }
    const int head = 2 * p + wn;
#pragma unroll
    for (int tt = 0; tt < 2; ++tt) {
        const int T = 4 * mt + 2 * wm + tt;
        char* q = ws + WS_Q + (size_t)(head * 64 + T) * 2048 + lane * 16;
        char* kv = ws + WS_KV + (size_t)(head * 64 + T) * 4096 + lane * 16;
        *(u32x4*)(q) = pack8(acc[tt][0], 0, QSCALE);
        *(u32x4*)(q + 1024) = pack8(acc[tt][0], 8, QSCALE);
        *(u32x4*)(kv) = pack8(acc[tt][1], 0, 1.f);
        *(u32x4*)(kv + 1024) = pack8(acc[tt][1], 8, 1.f);
        *(u32x4*)(kv + 2048) = pack8(acc[tt][2], 0, 1.f);
        *(u32x4*)(kv + 3072) = pack8(acc[tt][2], 8, 1.f);
    }
}

constexpr int AT_STAGE = 16384, AT_LDS = 3 * AT_STAGE;

__device__ __forceinline__ void attn_safe(const char* __restrict__ kvg, bf16x8 q0, bf16x8 q1, int lane, f32x16& o_out, float& l_out) {
    f32x16 o = (f32x16){};
    float m = -INFINITY, l = 0.f;
    for (int t = 0; t < 64; ++t) {
        const char* b = kvg + (size_t)t * 4096 + lane * 16;
        const bf16x8 k0 = *(const bf16x8*)(b), k1 = *(const bf16x8*)(b + 1024), v0 = *(const bf16x8*)(b + 2048), v1 = *(const bf16x8*)(b + 3072);
        f32x16 s = MFMA32(k0, q0, (f32x16){});
        s = MFMA32(k1, q1, s);
        float mx = s[0];
#pragma unroll
        for (int i = 1; i < 16; ++i) mx = fmaxf(mx, s[i]);
        mx = fmaxf(mx, __shfl_xor(mx, 32));
        const float mn = fmaxf(m, mx);
        const float alpha = (mn == -INFINITY) ? 1.f : __builtin_amdgcn_exp2f(m - mn);
        const float sub = (mn == -INFINITY) ? 0.f : mn;
        m = mn;
        float ps = 0.f;
#pragma unroll
        for (int i = 0; i < 16; ++i) { s[i] = __builtin_amdgcn_exp2f(s[i] - sub); }
        const u32x4 p0 = pack8(s, 0, 1.f), p1 = pack8(s, 8, 1.f);
#pragma unroll
        for (int i = 0; i < 4; ++i) ps += bf_lo(p0[i]) + bf_hi(p0[i]) + bf_lo(p1[i]) + bf_hi(p1[i]);
        l = l * alpha + ps;
#pragma unroll
        for (int i = 0; i < 16; ++i) o[i] *= alpha;
        o = MFMA32(v0, __builtin_bit_cast(bf16x8, p0), o);
        o = MFMA32(v1, __builtin_bit_cast(bf16x8, p1), o);
    }
    l += __shfl_xor(l, 32);
    o_out = o; l_out = l;
}

__global__ __launch_bounds__(512, 2) void k_attn(char* __restrict__ ws) {
    extern __shared__ __attribute__((aligned(16))) char smem[];
    lds_ptr lds = (lds_ptr)smem;
    const int tid = threadIdx.x, lane = tid & 63;
    const int wid = __builtin_amdgcn_readfirstlane(tid >> 6);
    const int xcd = blockIdx.x & 7, idx = blockIdx.x >> 3;
    const int head = 4 * xcd + (idx >> 3), qt = idx & 7;
    const int T = 8 * qt + wid;
    const char* kvg = ws + WS_KV + (size_t)head * 262144;
    const char* kvs = kvg + wid * 2048 + lane * 16;
    auto issue = [&](int st, int slot) {
        glds16(kvs + st * AT_STAGE, lds + slot * AT_STAGE + wid * 2048);
        glds16(kvs + st * AT_STAGE + 1024, lds + slot * AT_STAGE + wid * 2048 + 1024);
    };
    const char* qg = ws + WS_Q + (size_t)(head * 64 + T) * 2048 + lane * 16;
    const bf16x8 q0 = *(const bf16x8*)qg, q1 = *(const bf16x8*)(qg + 1024);
    asm volatile("" :: "v"(q0), "v"(q1));
    issue(0, 0); issue(1, 1);
    bf16x8 ones;
    { const int row = lane & 15, g = lane >> 4; const short v = (row == (g & 1)) ? (short)0x3f80 : (short)0;
#pragma unroll
      for (int i = 0; i < 8; ++i) ones[i] = v; }
    lds_ptr lp = lds + lane * 16;
    WAIT_VM(2); RAW_BAR(); SBAR();
    bf16x8 k0 = lds_rd128(lp), k1 = lds_rd128(lp + 1024);
    f32x16 sA = MFMA32(k0, q0, (f32x16){}); sA = MFMA32(k1, q1, sA);
    f32x16 negm;
    {
        float mx = sA[0];
#pragma unroll
        for (int i = 1; i < 16; ++i) mx = fmaxf(mx, sA[i]);
        auto rr = __builtin_amdgcn_permlane32_swap(__float_as_uint(mx), __float_as_uint(mx), false, false);
        mx = fmaxf(__uint_as_float(rr[0]), __uint_as_float(rr[1]));
#pragma unroll
        for (int i = 0; i < 16; ++i) { negm[i] = -mx; sA[i] -= mx; }
    }
    k0 = lds_rd128(lp + 4096); k1 = lds_rd128(lp + 4096 + 1024);
    bf16x8 v0 = lds_rd128(lp + 2048), v1 = lds_rd128(lp + 3072);
    f32x16 o = (f32x16){}, sB;
    f32x4 lsum = (f32x4){};

#define ASTEP(SC, SN, kp, vp, DOQK, DOK, DOV) do {                                               \
        if (DOQK) { SN = MFMA32(k0, q0, negm); SN = MFMA32(k1, q1, SN); }                          \
        if (DOK) { k0 = lds_rd128(kp); k1 = lds_rd128((kp) + 1024); }                              \
        _Pragma("unroll") for (int i = 0; i < 8; ++i) SC[i] = __builtin_amdgcn_exp2f(SC[i]);       \
        const u32x4 p0 = pack8(SC, 0, 1.f);                                                        \
        o = MFMA32(v0, __builtin_bit_cast(bf16x8, p0), o);                                         \
        lsum = MFMA16(ones, __builtin_bit_cast(bf16x8, p0), lsum);                                 \
        _Pragma("unroll") for (int i = 8; i < 16; ++i) SC[i] = __builtin_amdgcn_exp2f(SC[i]);      \
        const u32x4 p1 = pack8(SC, 8, 1.f);                                                        \
        o = MFMA32(v1, __builtin_bit_cast(bf16x8, p1), o);                                         \
        lsum = MFMA16(ones, __builtin_bit_cast(bf16x8, p1), lsum);                                 \
        if (DOV) { v0 = lds_rd128((vp) + 2048); v1 = lds_rd128((vp) + 3072); }                     \
    } while (0)

    int slot = 0;
    for (int j = 0; j < 15; ++j) {
        lds_ptr c = lp + slot * AT_STAGE;
        const int nslot = (slot == 2) ? 0 : slot + 1;
        lds_ptr n = lp + nslot * AT_STAGE;
        ASTEP(sA, sB, c + 2 * 4096, c + 1 * 4096, true, true, true);
        ASTEP(sB, sA, c + 3 * 4096, c + 2 * 4096, true, true, true);
        WAIT_VM(0); WAIT_LGKM0(); RAW_BAR(); SBAR();
        if (j + 2 < 16) issue(j + 2, (nslot == 2) ? 0 : nslot + 1);
        ASTEP(sA, sB, n, c + 3 * 4096, true, true, true);
        ASTEP(sB, sA, n + 4096, n, true, true, true);
        slot = nslot;
    }
    {
        lds_ptr c = lp + slot * AT_STAGE;
        ASTEP(sA, sB, c + 2 * 4096, c + 1 * 4096, true, true, true);
        ASTEP(sB, sA, c + 3 * 4096, c + 2 * 4096, true, true, true);
        ASTEP(sA, sB, c, c + 3 * 4096, true, false, true);
        ASTEP(sB, sA, c, c, false, false, false);
    }
    const int r = lane & 31;
    const float la = __shfl(lsum[0], r & 15), lb = __shfl(lsum[1], r & 15);
    float l = (r < 16) ? la : lb;
    bool bad = !(l < 1.0e30f);
#pragma unroll
    for (int i = 0; i < 16; ++i) bad |= !(fabsf(o[i]) < 1.0e37f);
    if (__any(bad)) attn_safe(kvg, q0, q1, lane, o, l);
    const float rl = 1.0f / l;
    char* og = ws + WS_O + (size_t)(T * 32 + head) * 2048 + lane * 16;
    *(u32x4*)(og) = pack8(o, 0, rl);
    *(u32x4*)(og + 1024) = pack8(o, 8, rl);
}

constexpr int G2_LDS = 131072;

__global__ __launch_bounds__(512, 2) void k_oproj(const char* __restrict__ ws, const char* __restrict__ wo, char* __restrict__ out) {
    extern __shared__ __attribute__((aligned(16))) char smem[];
    lds_ptr lds = (lds_ptr)smem;
    const int tid = threadIdx.x, lane = tid & 63;
    const int wid = __builtin_amdgcn_readfirstlane(tid >> 6);
    const int xcd = blockIdx.x & 7, idx = blockIdx.x >> 3;
    const int mt = 4 * (xcd & 3) + (idx & 3), nt = 8 * (xcd >> 2) + (idx >> 2);
    if (wid >= 4) {
        const int pw = wid - 4;
        const int sub = lane >> 5, row = (lane >> 2) & 7, cc = lane & 3;
        const char* wg = wo + (size_t)(8 * pw + row) * 2048 + nt * 128 + sub * 64 + cc * 16;
#pragma unroll
        for (int i = 0; i < 32; ++i) glds16(wg + (size_t)i * 32 * 2048, lds + (4 * i + pw) * 1024);
        WAIT_VM(24); RAW_BAR();
        WAIT_VM(16); RAW_BAR();
        WAIT_VM(8); RAW_BAR();
        WAIT_VM(0); RAW_BAR();
        return;
    }
    const int T = 4 * mt + wid;
    const int r = lane & 31, hh = lane >> 5;
    const char* og = ws + WS_O + (size_t)T * 65536 + lane * 16;
    lds_ptr wa = lds + (4 * hh + ((lane >> 2) & 3)) * 64 + ((lane >> 4) & 1) * 32 + (lane & 3) * 8;
    f32x16 acc0 = (f32x16){}, acc1 = (f32x16){};
    constexpr int PF = 8;
    bf16x8 of[PF];
#pragma unroll
    for (int i = 0; i < PF; ++i) of[i] = *(const bf16x8*)(og + i * 1024);
    for (int c = 0; c < 4; ++c) {
        RAW_BAR(); SBAR();
#pragma unroll
        for (int u = 0; u < 16; ++u) {
            const int kk = c * 16 + u;
            const bf16x8 a = of[u % PF];
            if (kk + PF < 64) of[u % PF] = *(const bf16x8*)(og + (size_t)(kk + PF) * 1024);
            lds_ptr b = wa + kk * 2048;
            const bf16x8 w0 = cat4(lds_tr(b), lds_tr(b + 1024));
            const bf16x8 w1 = cat4(lds_tr(b + 512), lds_tr(b + 1024 + 512));
            acc0 = MFMA32(w0, a, acc0);
            acc1 = MFMA32(w1, a, acc1);
        }
    }
    char* orow = out + (size_t)(32 * T + r) * 4096 + 2048 + nt * 128;
#pragma unroll
    for (int n2 = 0; n2 < 2; ++n2) {
        const f32x16& a = n2 ? acc1 : acc0;
#pragma unroll
        for (int g = 0; g < 4; g += 2) {
            unsigned ax = cvtpk(a[4 * g], a[4 * g + 1]), ay = cvtpk(a[4 * g + 2], a[4 * g + 3]);
            unsigned bx = cvtpk(a[4 * g + 4], a[4 * g + 5]), by = cvtpk(a[4 * g + 6], a[4 * g + 7]);
            auto r0 = __builtin_amdgcn_permlane32_swap(ax, bx, false, false);
            auto r1 = __builtin_amdgcn_permlane32_swap(ay, by, false, false);
            *(u32x4*)(orow + n2 * 64 + g * 16 + hh * 16) = (u32x4){r0[0], r1[0], r0[1], r1[1]};
        }
    }
}

constexpr int CV_HCOPY = 4160;
constexpr int CV_HCH = 4 * CV_HCOPY;
constexpr int CV_X = 4 * CV_HCH;
constexpr int CV_XCH = 95 * 80;
constexpr int CV_LDS = CV_X + 4 * CV_XCH;
constexpr int CV_YCH = 64 * 144;

__global__ __launch_bounds__(256, 1) void k_conv(const char* __restrict__ q, const char* __restrict__ k, const char* __restrict__ v,
                                                 const unsigned short* __restrict__ shk, const unsigned short* __restrict__ ssmk,
                                                 const unsigned short* __restrict__ ssmd, char* __restrict__ out) {
    extern __shared__ __attribute__((aligned(16))) char smem[];
    lds_ptr lds = (lds_ptr)smem;
    const int tid = threadIdx.x, lane = tid & 63;
    const int wid = __builtin_amdgcn_readfirstlane(tid >> 6);
    const int xcd = blockIdx.x & 7, idx = blockIdx.x >> 3;
    const int ch0 = 128 * xcd + 4 * idx;
    {
        const char* hrow = (const char*)(ssmk + (size_t)(ch0 + wid) * 2048);
#pragma unroll
        for (int cp = 0; cp < 4; ++cp)
#pragma unroll
            for (int pc = 0; pc < 4; ++pc) {
                const int vv = 4 + pc * 64 + lane;
                int e = 8 * vv - 31 + cp;
                if (e > 2040) e = 2040;
                glds16(hrow + 2 * e, lds + wid * CV_HCH + cp * CV_HCOPY + (4 + pc * 64) * 16);
            }
    }
    const int t0 = 8 * tid;
    u32x2 kr[11], vr[8];
#pragma unroll
    for (int i = 0; i < 11; ++i) {
        const int t = t0 - 3 + i;
        kr[i] = (t >= 0) ? *(const u32x2*)(k + (size_t)t * 2048 + ch0 * 2) : (u32x2){0u, 0u};
    }
#pragma unroll
    for (int i = 0; i < 8; ++i) vr[i] = *(const u32x2*)(v + (size_t)(t0 + i) * 2048 + ch0 * 2);
    float sw[4][4], dv[4];
#pragma unroll
    for (int c = 0; c < 4; ++c) {
#pragma unroll
        for (int j = 0; j < 4; ++j) sw[c][j] = __uint_as_float((unsigned)shk[(ch0 + c) * 4 + j] << 16);
        dv[c] = __uint_as_float((unsigned)ssmd[ch0 + c] << 16);
    }
    for (int i = tid; i < 4 * 155; i += 256) { const int c = i / 155, o = i % 155; *(LAS u32x4*)(lds + CV_X + c * CV_XCH + o * 16) = (u32x4){0u, 0u, 0u, 0u}; }
    unsigned kvp[4][4];
#pragma unroll
    for (int c = 0; c < 4; ++c) {
        float kf[11], kvf[8];
#pragma unroll
        for (int i = 0; i < 11; ++i) kf[i] = (c & 1) ? bf_hi(kr[i][c >> 1]) : bf_lo(kr[i][c >> 1]);
#pragma unroll
        for (int i = 0; i < 8; ++i) {
            float a = rbf(kf[i] * sw[c][3]);
            a = rbf(a + rbf(kf[i + 1] * sw[c][2]));
            a = rbf(a + rbf(kf[i + 2] * sw[c][1]));
            a = rbf(a + rbf(kf[i + 3] * sw[c][0]));
            const float vf = (c & 1) ? bf_hi(vr[i][c >> 1]) : bf_lo(vr[i][c >> 1]);
            kvf[i] = a * vf;
        }
#pragma unroll
        for (int j = 0; j < 4; ++j) kvp[c][j] = cvtpk(kvf[2 * j], kvf[2 * j + 1]);
        const u32x4 rev = (u32x4){cvtpk(kvf[7], kvf[6]), cvtpk(kvf[5], kvf[4]), cvtpk(kvf[3], kvf[2]), cvtpk(kvf[1], kvf[0])};
        *(LAS u32x4*)(lds + CV_X + c * CV_XCH + ((tid >> 2) + 31) * 80 + (tid & 3) * 16) = rev;
    }
    WAIT_VM(0);
    if (lane < 20) {
        const int cp = lane / 5, which = lane % 5, vv = (which < 4) ? which : 259;
        const unsigned short* hrow = ssmk + (size_t)(ch0 + wid) * 2048;
        unsigned e[8];
#pragma unroll
        for (int j = 0; j < 8; ++j) { const int i = 8 * vv + j - 31 + cp; e[j] = (i >= 0 && i < 2048) ? (unsigned)hrow[i] : 0u; }
        *(LAS u32x4*)(lds + wid * CV_HCH + cp * CV_HCOPY + vv * 16) = (u32x4){e[0] | (e[1] << 16), e[2] | (e[3] << 16), e[4] | (e[5] << 16), e[6] | (e[7] << 16)};
    }
    __syncthreads();
    const int r = lane & 31, hh = lane >> 5;
    f32x16 y0 = (f32x16){}, y1 = (f32x16){};
    {
        lds_ptr ha = lds + wid * CV_HCH + (r & 3) * CV_HCOPY + 48 - 16 * hh + 8 * (r >> 2);
        lds_ptr hb = ha + 8; asm volatile("" : "+v"(hb));
        lds_ptr xb = lds + CV_X + wid * CV_XCH + (r + 31) * 80 + hh * 16;
        auto ldA = [&](int d) { return cat4(*(const LAS s16x4*)(ha + 32 * d), *(const LAS s16x4*)(hb + 32 * d)); };
#pragma unroll 4
        for (int m = 0; m < 32; ++m) {
            const bf16x8 a0 = ldA(2 * m), a1 = ldA(2 * m - 1), a2 = ldA(2 * m + 64), a3 = ldA(2 * m + 63);
            lds_ptr xp = xb - 80 * m;
            const bf16x8 b00 = lds_rd128(xp), b01 = lds_rd128(xp + 32), b10 = lds_rd128(xp + 2560), b11 = lds_rd128(xp + 2560 + 32);
            y0 = MFMA32(a0, b00, y0); y1 = MFMA32(a0, b10, y1);
            y0 = MFMA32(a1, b01, y0); y1 = MFMA32(a1, b11, y1);
            y1 = MFMA32(a2, b00, y1); y1 = MFMA32(a3, b01, y1);
        }
    }
    __syncthreads();
    {
        lds_ptr yb = lds + wid * CV_YCH + r * 144 + hh * 16;
#pragma unroll
        for (int g = 0; g < 4; ++g) {
            *(LAS f32x4*)(yb + g * 32) = (f32x4){y0[4 * g], y0[4 * g + 1], y0[4 * g + 2], y0[4 * g + 3]};
            *(LAS f32x4*)(yb + 32 * 144 + g * 32) = (f32x4){y1[4 * g], y1[4 * g + 1], y1[4 * g + 2], y1[4 * g + 3]};
        }
    }
    u32x2 qr[8];
#pragma unroll
    for (int i = 0; i < 8; ++i) qr[i] = *(const u32x2*)(q + (size_t)(t0 + i) * 2048 + ch0 * 2);
    __syncthreads();
    float res[4][8];
#pragma unroll
    for (int c = 0; c < 4; ++c) {
        lds_ptr yp = lds + c * CV_YCH + (tid >> 2) * 144 + (tid & 3) * 32;
        const f32x4 ya = *(const LAS f32x4*)yp, yb = *(const LAS f32x4*)(yp + 16);
#pragma unroll
        for (int i = 0; i < 8; ++i) {
            const float y = rbf(i < 4 ? ya[i] : yb[i - 4]);
            const float kvv = (i & 1) ? bf_hi(kvp[c][i >> 1]) : bf_lo(kvp[c][i >> 1]);
            const float z = rbf(y + rbf(kvv * dv[c]));
            const float qv = (c & 1) ? bf_hi(qr[i][c >> 1]) : bf_lo(qr[i][c >> 1]);
            res[c][i] = qv * z;
        }
    }
#pragma unroll
    for (int i = 0; i < 8; ++i)
        *(u32x2*)(out + (size_t)(t0 + i) * 4096 + ch0 * 2) = (u32x2){cvtpk(res[0][i], res[1][i]), cvtpk(res[2][i], res[3][i])};
}

extern "C" void kernel_launch(void* const* d_in, const int* in_sizes, int n_in, void* d_out, int out_size, void* d_ws, size_t ws_size,
                              hipStream_t stream) {
    const char* A_q = (const char*)d_in[0];
    const char* A_k = (const char*)d_in[1];
    const char* A_v = (const char*)d_in[2];
    const unsigned short* A_shk = (const unsigned short*)d_in[3];
    const unsigned short* A_ssmk = (const unsigned short*)d_in[4];
    const unsigned short* A_ssmd = (const unsigned short*)d_in[5];
    const char* B_x = (const char*)d_in[6];
    const char* B_wqkv = (const char*)d_in[7];
    const char* B_wo = (const char*)d_in[8];
    char* ws = (char*)d_ws;
    char* out = (char*)d_out;
    (void)hipFuncSetAttribute((const void*)k_qkv, hipFuncAttributeMaxDynamicSharedMemorySize, G1_LDS);
    (void)hipFuncSetAttribute((const void*)k_attn, hipFuncAttributeMaxDynamicSharedMemorySize, AT_LDS);
    (void)hipFuncSetAttribute((const void*)k_oproj, hipFuncAttributeMaxDynamicSharedMemorySize, G2_LDS);
    (void)hipFuncSetAttribute((const void*)k_conv, hipFuncAttributeMaxDynamicSharedMemorySize, CV_LDS);
    k_qkv<<<256, 512, G1_LDS, stream>>>(B_x, B_wqkv, ws);
    k_conv<<<256, 256, CV_LDS, stream>>>(A_q, A_k, A_v, A_shk, A_ssmk, A_ssmd, out);
    k_attn<<<256, 512, AT_LDS, stream>>>(ws);
    k_oproj<<<256, 512, G2_LDS, stream>>>(ws, B_wo, out);
}
```

```cpp
#include <hip/hip_runtime.h>
#include <hip/hip_bf16.h>
#include <stdint.h>

typedef short bf16x8 __attribute__((ext_vector_type(8)));
typedef short s16x4 __attribute__((ext_vector_type(4)));
typedef float f32x16 __attribute__((ext_vector_type(16)));
typedef float f32x4 __attribute__((ext_vector_type(4)));
typedef float f32x2 __attribute__((ext_vector_type(2)));
typedef __bf16 bf16x2n __attribute__((ext_vector_type(2)));
typedef unsigned u32x4 __attribute__((ext_vector_type(4)));
typedef unsigned u32x2 __attribute__((ext_vector_type(2)));
typedef short v4i16_t __attribute__((ext_vector_type(4)));
#define LAS __attribute__((address_space(3)))
typedef LAS char* lds_ptr;

#define SBAR() __builtin_amdgcn_sched_barrier(0)
#define MFMA32(a, b, c) __builtin_amdgcn_mfma_f32_32x32x16_bf16(a, b, c, 0, 0, 0)
#define MFMA16(a, b, c) __builtin_amdgcn_mfma_f32_16x16x32_bf16(a, b, c, 0, 0, 0)
#define WAIT_VM(n) asm volatile("s_waitcnt vmcnt(" #n ")" ::: "memory")
#define WAIT_LGKM0() asm volatile("s_waitcnt lgkmcnt(0)" ::: "memory")
#define RAW_BAR() __builtin_amdgcn_s_barrier()
#define BC8(x) __builtin_bit_cast(bf16x8, x)

#define TS(kid, slot) do {} while (0)

constexpr float QSCALE = 0.17677669529663687f * 1.4426950408889634f;

constexpr unsigned WS_Q = 0;
constexpr unsigned WS_KV = 4u << 20;
constexpr unsigned WS_O = 12u << 20;
constexpr unsigned WS_X = 16u << 20;
constexpr unsigned WS_Y = 20u << 20;
constexpr unsigned WS_USED = 24u << 20;

__device__ __forceinline__ unsigned cvtpk(float lo, float hi) {
    return __builtin_bit_cast(unsigned, __builtin_convertvector((f32x2){lo, hi}, bf16x2n));
}
__device__ __forceinline__ float bf_lo(unsigned w) { return __uint_as_float(w << 16); }
__device__ __forceinline__ float bf_hi(unsigned w) { return __uint_as_float(w & 0xffff0000u); }
typedef __amdgpu_buffer_rsrc_t rsrc_t;
__device__ __forceinline__ rsrc_t mk_rsrc(const void* p, unsigned bytes) { return __builtin_amdgcn_make_buffer_rsrc((void*)p, 0, (int)bytes, 0x00020000); }
__device__ __forceinline__ void bdma16(rsrc_t r, unsigned voff, unsigned soff, lds_ptr l) {
    __builtin_amdgcn_raw_ptr_buffer_load_lds(r, (LAS void*)l, 16, (int)voff, (int)soff, 0, 0);
}
__device__ __forceinline__ bf16x8 lds_rd128(lds_ptr p) { return *(const LAS bf16x8*)p; }
__device__ __forceinline__ s16x4 lds_rd64(lds_ptr p) { return *(const LAS s16x4*)p; }
__device__ __forceinline__ s16x4 lds_tr(lds_ptr p) {
    return __builtin_bit_cast(s16x4, __builtin_amdgcn_ds_read_tr16_b64_v4i16((LAS v4i16_t*)p));
}
__device__ __forceinline__ bf16x8 cat4(s16x4 a, s16x4 b) { return (bf16x8){a[0], a[1], a[2], a[3], b[0], b[1], b[2], b[3]}; }
__device__ __forceinline__ u32x4 pack8(const f32x16& v, int o, float sc) {
    return (u32x4){cvtpk(v[o] * sc, v[o + 1] * sc), cvtpk(v[o + 2] * sc, v[o + 3] * sc), cvtpk(v[o + 4] * sc, v[o + 5] * sc), cvtpk(v[o + 6] * sc, v[o + 7] * sc)};
}
__device__ __forceinline__ u32x4 rows8(const f32x16& a, int g) {
    unsigned ax = cvtpk(a[4 * g], a[4 * g + 1]), ay = cvtpk(a[4 * g + 2], a[4 * g + 3]);
    unsigned bx = cvtpk(a[4 * g + 4], a[4 * g + 5]), by = cvtpk(a[4 * g + 6], a[4 * g + 7]);
    auto r0 = __builtin_amdgcn_permlane32_swap(ax, bx, false, false);
    auto r1 = __builtin_amdgcn_permlane32_swap(ay, by, false, false);
    return (u32x4){r0[0], r1[0], r0[1], r1[1]};
}

__device__ __forceinline__ void ssm_prep(const char* __restrict__ k, const char* __restrict__ v, const unsigned short* __restrict__ shk,
                                         char* __restrict__ ws, int cg, int tr, int tid) {
    const int cp = tid & 31, tg = tid >> 5;
    const int ch = 64 * cg + 2 * cp, t0 = 128 * tr + 8 * tg;
    unsigned kr[11], vr[8];
#pragma unroll
    for (int i = 0; i < 11; ++i) {
        const int t = t0 - 3 + i;
        const unsigned x = *(const unsigned*)(k + (size_t)(t < 0 ? 0 : t) * 2048 + ch * 2);
        kr[i] = (i < 3 && t < 0) ? 0u : x;
    }
#pragma unroll
    for (int i = 0; i < 8; ++i) vr[i] = *(const unsigned*)(v + (size_t)(t0 + i) * 2048 + ch * 2);
    const u32x4 sw = *(const u32x4*)(shk + ch * 4);
#pragma unroll
    for (int c = 0; c < 2; ++c) {
        const float w0 = bf_lo(sw[2 * c]), w1 = bf_hi(sw[2 * c]), w2 = bf_lo(sw[2 * c + 1]), w3 = bf_hi(sw[2 * c + 1]);
        float kf[11], kvf[8];
#pragma unroll
        for (int i = 0; i < 11; ++i) kf[i] = c ? bf_hi(kr[i]) : bf_lo(kr[i]);
#pragma unroll
        for (int i = 0; i < 8; ++i) {
            const float s = kf[i] * w3 + kf[i + 1] * w2 + kf[i + 2] * w1 + kf[i + 3] * w0;
            kvf[i] = s * (c ? bf_hi(vr[i]) : bf_lo(vr[i]));
        }
        const u32x4 rev = (u32x4){cvtpk(kvf[7], kvf[6]), cvtpk(kvf[5], kvf[4]), cvtpk(kvf[3], kvf[2]), cvtpk(kvf[1], kvf[0])};
        *(u32x4*)(ws + WS_X + (size_t)(ch + c) * 4096 + t0 * 2) = rev;
    }
}

__device__ __forceinline__ void ssm_epi(const char* __restrict__ q, const char* __restrict__ ws, char* __restrict__ out, int cg, int tr, int tid) {
    const int cp = tid & 31, tg = tid >> 5;
    const int ch = 64 * cg + 2 * cp, t0 = 128 * tr + 8 * tg;
    const u32x4 y0 = *(const u32x4*)(ws + WS_Y + (size_t)ch * 4096 + t0 * 2);
    const u32x4 y1 = *(const u32x4*)(ws + WS_Y + (size_t)(ch + 1) * 4096 + t0 * 2);
    unsigned qr[8];
#pragma unroll
    for (int i = 0; i < 8; ++i) qr[i] = *(const unsigned*)(q + (size_t)(t0 + i) * 2048 + ch * 2);
#pragma unroll
    for (int i = 0; i < 8; ++i) {
        const float a = (i & 1) ? bf_hi(y0[i >> 1]) : bf_lo(y0[i >> 1]);
        const float b = (i & 1) ? bf_hi(y1[i >> 1]) : bf_lo(y1[i >> 1]);
        *(unsigned*)(out + (size_t)(t0 + i) * 4096 + ch * 2) = cvtpk(bf_lo(qr[i]) * a, bf_hi(qr[i]) * b);
    }
}

constexpr int G1_STAGE = 40960, G1_WOFF = 16384, G1_LDS = 3 * G1_STAGE;

__global__ __launch_bounds__(512, 2) void k_front(const char* __restrict__ x, const char* __restrict__ w, const char* __restrict__ ak,
                                                  const char* __restrict__ av, const unsigned short* __restrict__ shk, char* __restrict__ ws) {
    extern __shared__ __attribute__((aligned(16))) char smem[];
    lds_ptr lds = (lds_ptr)smem;
    const int tid = threadIdx.x, lane = tid & 63;
    const int wid = __builtin_amdgcn_readfirstlane(tid >> 6);
    const int xcd = blockIdx.x & 7, idx = blockIdx.x >> 3;
    const int mt = 8 * (xcd & 1) + (idx & 7), p = 4 * (xcd >> 1) + (idx >> 3);
    TS(0, 0);
    if (wid >= 4) {
        const int pw = wid - 4;
        const rsrc_t xr = mk_rsrc(x, 2048 * 2048), wr = mk_rsrc(w, 1024 * 6144);
        const int rl = lane >> 3, c = lane & 7;
        const unsigned xv0 = (128 * mt + 32 * pw + rl) * 2048 + ((c ^ (lane >> 4)) << 4);
        const unsigned xv1 = (128 * mt + 32 * pw + rl) * 2048 + ((c ^ (4 + (lane >> 4))) << 4);
        const unsigned wv = (16 * pw + ((lane >> 2) & 7)) * 6144 + p * 128 + (lane >> 5) * 64 + (lane & 3) * 16;
        auto issue = [&](int kt, int slot) {
            lds_ptr sb = lds + slot * G1_STAGE;
            bdma16(xr, xv0, kt * 128, sb + (4 * pw + 0) * 1024);
            bdma16(xr, xv1, kt * 128 + 8 * 2048, sb + (4 * pw + 1) * 1024);
            bdma16(xr, xv0, kt * 128 + 16 * 2048, sb + (4 * pw + 2) * 1024);
            bdma16(xr, xv1, kt * 128 + 24 * 2048, sb + (4 * pw + 3) * 1024);
#pragma unroll
            for (int kb = 0; kb < 2; ++kb)
#pragma unroll
                for (int seg = 0; seg < 3; ++seg)
                    bdma16(wr, wv, kt * 64 * 6144 + kb * 8 * 6144 + seg * 2048, sb + G1_WOFF + ((2 * pw + kb) * 3 + seg) * 1024);
        };
        issue(0, 0); issue(1, 1); issue(2, 2);
        ssm_prep(ak, av, shk, ws, 2 * xcd + (idx & 1), idx >> 1, tid);
        WAIT_VM(20); RAW_BAR();
        int slot = 0;
        for (int j = 1; j < 16; ++j) {
            if (j + 1 < 16) { WAIT_VM(10); } else { WAIT_VM(0); }
            RAW_BAR();
            if (j + 2 < 16) issue(j + 2, slot);
            slot = (slot == 2) ? 0 : slot + 1;
        }
        return;
    }
    ssm_prep(ak, av, shk, ws, 2 * xcd + (idx & 1), idx >> 1, tid);
    const int wm = wid >> 1, wn = wid & 1;
    const int r = lane & 31, hh = lane >> 5;
    int xa[4];
#pragma unroll
    for (int ks = 0; ks < 4; ++ks) xa[ks] = (64 * wm + r) * 128 + (((2 * ks + hh) ^ ((r >> 1) & 7)) << 4);
    const int wa = G1_WOFF + hh * 3072 + wn * 512 + ((lane >> 2) & 3) * 64 + ((lane >> 4) & 1) * 32 + (lane & 3) * 8;
    f32x16 acc[2][3];
#pragma unroll
    for (int i = 0; i < 2; ++i)
#pragma unroll
        for (int g = 0; g < 3; ++g) acc[i][g] = (f32x16){};
    bf16x8 xf[2][2];
    s16x4 wl[2][3], wh[2][3];
#define G1_STEP(C, N, SB, KS, BARRIER, LOAD) do {                                                                  \
        acc[0][0] = MFMA32(cat4(wl[C][0], wh[C][0]), xf[C][0], acc[0][0]); SBAR();                                   \
        acc[0][1] = MFMA32(cat4(wl[C][1], wh[C][1]), xf[C][0], acc[0][1]); SBAR();                                   \
        if (BARRIER) { WAIT_LGKM0(); RAW_BAR(); SBAR(); }                                                            \
        acc[0][2] = MFMA32(xf[C][0], cat4(wl[C][2], wh[C][2]), acc[0][2]); SBAR();                                   \
        if (LOAD) { wl[N][0] = lds_tr((SB) + wa + (KS) * 6144); wh[N][0] = lds_tr((SB) + wa + (KS) * 6144 + 256);    \
                    xf[N][0] = lds_rd128((SB) + xa[KS]); } SBAR();                                                   \
        acc[1][0] = MFMA32(cat4(wl[C][0], wh[C][0]), xf[C][1], acc[1][0]); SBAR();                                   \
        if (LOAD) { wl[N][1] = lds_tr((SB) + wa + (KS) * 6144 + 1024); wh[N][1] = lds_tr((SB) + wa + (KS) * 6144 + 1280); \
                    wl[N][2] = lds_tr((SB) + wa + (KS) * 6144 + 2048); } SBAR();                                     \
        acc[1][1] = MFMA32(cat4(wl[C][1], wh[C][1]), xf[C][1], acc[1][1]); SBAR();                                   \
        if (LOAD) { wh[N][2] = lds_tr((SB) + wa + (KS) * 6144 + 2304); xf[N][1] = lds_rd128((SB) + xa[KS] + 4096); } SBAR(); \
        acc[1][2] = MFMA32(xf[C][1], cat4(wl[C][2], wh[C][2]), acc[1][2]); SBAR();                                   \
    } while (0)
    RAW_BAR();
    SBAR();
    TS(0, 1);
    xf[0][0] = lds_rd128(lds + xa[0]); xf[0][1] = lds_rd128(lds + xa[0] + 4096);
#pragma unroll
    for (int g = 0; g < 3; ++g) { wl[0][g] = lds_tr(lds + wa + g * 1024); wh[0][g] = lds_tr(lds + wa + g * 1024 + 256); }
    SBAR();
    int slot = 0;
    for (int kt = 0; kt < 15; ++kt) {
        lds_ptr sb = lds + slot * G1_STAGE;
        slot = (slot == 2) ? 0 : slot + 1;
        lds_ptr sn = lds + slot * G1_STAGE;
        G1_STEP(0, 1, sb, 1, false, true);
        G1_STEP(1, 0, sb, 2, false, true);
        G1_STEP(0, 1, sb, 3, false, true);
        G1_STEP(1, 0, sn, 0, true, true);
    }
    {
        lds_ptr sb = lds + slot * G1_STAGE;
        G1_STEP(0, 1, sb, 1, false, true);
        G1_STEP(1, 0, sb, 2, false, true);
        G1_STEP(0, 1, sb, 3, false, true);
        G1_STEP(1, 0, sb, 0, false, false);
    }
    TS(0, 2);
    const int head = 2 * p + wn;
#pragma unroll
    for (int tt = 0; tt < 2; ++tt) {
        const int T = 4 * mt + 2 * wm + tt;
        char* q = ws + WS_Q + (size_t)(head * 64 + T) * 2048 + lane * 16;
        char* kv = ws + WS_KV + (size_t)(head * 64 + T) * 4096 + lane * 16;
        *(u32x4*)(q) = pack8(acc[tt][0], 0, QSCALE);
        *(u32x4*)(q + 1024) = pack8(acc[tt][0], 8, QSCALE);
        *(u32x4*)(kv) = pack8(acc[tt][1], 0, 1.f);
        *(u32x4*)(kv + 1024) = pack8(acc[tt][1], 8, 1.f);
        *(u32x4*)(kv + 2048) = pack8(acc[tt][2], 0, 1.f);
        *(u32x4*)(kv + 3072) = pack8(acc[tt][2], 8, 1.f);
    }
    TS(0, 3);
}

constexpr int CV_HCOPY = 4160;
constexpr int CV_HCH = 4 * CV_HCOPY;
constexpr int CV_X = 4 * CV_HCH;
constexpr int CV_XCH = 95 * 80;
constexpr int AT_OFF = 98304;
constexpr int AT_STAGE = 16384;
constexpr int MID_LDS = AT_OFF + 3 * AT_STAGE;

__device__ __forceinline__ void attn_safe(const char* __restrict__ kvg, bf16x8 q0, bf16x8 q1, int lane, f32x16& o_out, float& l_out) {
    f32x16 o = (f32x16){};
    float m = -INFINITY, l = 0.f;
    for (int t = 0; t < 64; ++t) {
        const char* b = kvg + (size_t)t * 4096 + lane * 16;
        const bf16x8 k0 = *(const bf16x8*)(b), k1 = *(const bf16x8*)(b + 1024), v0 = *(const bf16x8*)(b + 2048), v1 = *(const bf16x8*)(b + 3072);
        f32x16 s = MFMA32(k0, q0, (f32x16){});
        s = MFMA32(k1, q1, s);
        float mx = s[0];
#pragma unroll
        for (int i = 1; i < 16; ++i) mx = fmaxf(mx, s[i]);
        mx = fmaxf(mx, __shfl_xor(mx, 32));
        const float mn = fmaxf(m, mx);
        const float alpha = (mn == -INFINITY) ? 1.f : __builtin_amdgcn_exp2f(m - mn);
        const float sub = (mn == -INFINITY) ? 0.f : mn;
        m = mn;
        float ps = 0.f;
#pragma unroll
        for (int i = 0; i < 16; ++i) { s[i] = __builtin_amdgcn_exp2f(s[i] - sub); }
        const u32x4 p0 = pack8(s, 0, 1.f), p1 = pack8(s, 8, 1.f);
#pragma unroll
        for (int i = 0; i < 4; ++i) ps += bf_lo(p0[i]) + bf_hi(p0[i]) + bf_lo(p1[i]) + bf_hi(p1[i]);
        l = l * alpha + ps;
#pragma unroll
        for (int i = 0; i < 16; ++i) o[i] *= alpha;
        o = MFMA32(v0, BC8(p0), o);
        o = MFMA32(v1, BC8(p1), o);
    }
    l += __shfl_xor(l, 32);
    o_out = o; l_out = l;
}

__global__ __launch_bounds__(512, 2) void k_mid(const unsigned short* __restrict__ ssmk, const unsigned short* __restrict__ ssmd, char* __restrict__ ws) {
    extern __shared__ __attribute__((aligned(16))) char smem[];
    lds_ptr lds = (lds_ptr)smem;
    const int tid = threadIdx.x, lane = tid & 63;
    const int wid = __builtin_amdgcn_readfirstlane(tid >> 6);
    const int xcd = blockIdx.x & 7, idx = blockIdx.x >> 3;
    const int head = 4 * xcd + (idx >> 3), qt = idx & 7;
    const int T = 8 * qt + wid;
    const int r = lane & 31, hh = lane >> 5;
    TS(1, 0);
    const char* kvg = ws + WS_KV + (size_t)head * 262144;
    const rsrc_t kvr = mk_rsrc(kvg, 262144);
    const unsigned kvo = wid * 2048 + lane * 16;
    lds_ptr ring = lds + AT_OFF;
    auto issue = [&](int st, int slot) {
        bdma16(kvr, kvo, st * AT_STAGE, ring + slot * AT_STAGE + wid * 2048);
        bdma16(kvr, kvo + 1024, st * AT_STAGE, ring + slot * AT_STAGE + wid * 2048 + 1024);
    };
    const char* qg = ws + WS_Q + (size_t)(head * 64 + T) * 2048 + lane * 16;
    const bf16x8 q0 = *(const bf16x8*)qg, q1 = *(const bf16x8*)(qg + 1024);

    {
        const int cc = wid & 3, half = wid >> 2;
        const int ch = 128 * xcd + 4 * idx + cc;
        lds_ptr hb = lds + cc * CV_HCH;
        lds_ptr xb0 = lds + CV_X + cc * CV_XCH;
        {
            const rsrc_t hr = mk_rsrc(ssmk, 1024 * 4096);
#pragma unroll
            for (int c2 = 0; c2 < 2; ++c2) {
                const int cp = 2 * half + c2;
                const unsigned hv = ch * 4096 + 2 * (8 * (4 + lane) - 31 + cp);
#pragma unroll
                for (int pc = 0; pc < 4; ++pc) bdma16(hr, hv, pc * 1024, hb + cp * CV_HCOPY + 64 + pc * 1024);
            }
            const rsrc_t xr = mk_rsrc(ws + WS_X, 4u << 20);
#pragma unroll
            for (int i = 0; i < 3; ++i) {
                const int ii = 3 * half + i;
                if (ii < 5) {
                    const int s = ii * 64 + lane, col = s / 5, pc = s - 5 * col;
                    bdma16(xr, ch * 4096 + col * 64 + (pc > 3 ? 3 : pc) * 16, 0, xb0 + 31 * 80 + ii * 1024);
                }
            }
        }
        issue(0, 0); issue(1, 1);
        for (int i = tid; i < 4 * 155; i += 512) { const int c = i / 155, o = i - 155 * c; *(LAS u32x4*)(lds + CV_X + c * CV_XCH + o * 16) = (u32x4){0u, 0u, 0u, 0u}; }
        unsigned e[8];
        const int fcp = 2 * half + lane / 5, fw = lane % 5, fv = (fw < 4) ? fw : 259;
        if (lane < 10) {
            const unsigned short* hrow = ssmk + (size_t)ch * 2048;
#pragma unroll
            for (int j = 0; j < 8; ++j) { const int i = 8 * fv + j - 31 + fcp; e[j] = (i >= 0 && i < 2048) ? (unsigned)hrow[i] : 0u; }
        }
        const unsigned dbits = ssmd[ch];
        WAIT_VM(0);
        if (lane < 10)
            *(LAS u32x4*)(hb + fcp * CV_HCOPY + fv * 16) = (u32x4){e[0] | (e[1] << 16), e[2] | (e[3] << 16), e[4] | (e[5] << 16), e[6] | (e[7] << 16)};
        WAIT_LGKM0(); RAW_BAR(); SBAR();
        TS(1, 1);
        f32x16 y0 = (f32x16){}, y1 = (f32x16){};
        lds_ptr ha = hb + (r & 3) * CV_HCOPY + 48 - 16 * hh + 8 * (r >> 2) + half * 1024;
        lds_ptr hc = ha + 8; asm volatile("" : "+v"(hc));
        lds_ptr xb = xb0 + (r + 31 - 16 * half) * 80 + hh * 16;
        if (half == 0) {
            bf16x8 dk[2];
#pragma unroll
            for (int ks = 0; ks < 2; ++ks) {
                const int j = 16 * ks + 8 * hh + 7 - r;
                u32x4 d;
#pragma unroll
                for (int jj = 0; jj < 4; ++jj) d[jj] = (j == 2 * jj) ? dbits : ((j == 2 * jj + 1) ? (dbits << 16) : 0u);
                dk[ks] = BC8(d);
            }
            y0 = MFMA32(dk[0], lds_rd128(xb), y0); y1 = MFMA32(dk[0], lds_rd128(xb + 2560), y1);
            y0 = MFMA32(dk[1], lds_rd128(xb + 32), y0); y1 = MFMA32(dk[1], lds_rd128(xb + 2560 + 32), y1);
        }
        s16x4 al[2][4], ah[2][4];
        bf16x8 bb[2][4];
#define CV_LDA(N, i, D) do { al[N][i] = lds_rd64(ha + 32 * (D)); ah[N][i] = lds_rd64(hc + 32 * (D)); } while (0)
#define CV_STEP(C, N, M1, LOAD) do {                                                                               \
        y0 = MFMA32(cat4(al[C][0], ah[C][0]), bb[C][0], y0); SBAR();                                                 \
        if (LOAD) { CV_LDA(N, 0, 2 * (M1)); } SBAR();                                                                \
        y1 = MFMA32(cat4(al[C][0], ah[C][0]), bb[C][2], y1); SBAR();                                                 \
        if (LOAD) { bb[N][0] = lds_rd128(xb - 80 * (M1)); bb[N][2] = lds_rd128(xb - 80 * (M1) + 2560); } SBAR();     \
        y0 = MFMA32(cat4(al[C][1], ah[C][1]), bb[C][1], y0); SBAR();                                                 \
        if (LOAD) { CV_LDA(N, 1, 2 * (M1) - 1); } SBAR();                                                            \
        y1 = MFMA32(cat4(al[C][1], ah[C][1]), bb[C][3], y1); SBAR();                                                 \
        if (LOAD) { bb[N][1] = lds_rd128(xb - 80 * (M1) + 32); bb[N][3] = lds_rd128(xb - 80 * (M1) + 2560 + 32); } SBAR(); \
        y1 = MFMA32(cat4(al[C][2], ah[C][2]), bb[C][0], y1); SBAR();                                                 \
        if (LOAD) { CV_LDA(N, 2, 2 * (M1) + 64); } SBAR();                                                           \
        y1 = MFMA32(cat4(al[C][3], ah[C][3]), bb[C][1], y1); SBAR();                                                 \
        if (LOAD) { CV_LDA(N, 3, 2 * (M1) + 63); } SBAR();                                                           \
    } while (0)
        CV_LDA(0, 0, 0); bb[0][0] = lds_rd128(xb); bb[0][2] = lds_rd128(xb + 2560);
        CV_LDA(0, 1, -1); bb[0][1] = lds_rd128(xb + 32); bb[0][3] = lds_rd128(xb + 2560 + 32);
        CV_LDA(0, 2, 64); CV_LDA(0, 3, 63);
        SBAR();
#pragma unroll
        for (int m = 0; m < 16; m += 2) {
            CV_STEP(0, 1, m + 1, true);
            CV_STEP(1, 0, m + 2, (m + 2 < 16));
        }
        TS(1, 2);
        RAW_BAR(); SBAR();
        {
            lds_ptr ex = lds + wid * 4096 + lane * 16;
            const f32x16& snd = half ? y0 : y1;
#pragma unroll
            for (int g = 0; g < 4; ++g) *(LAS f32x4*)(ex + g * 1024) = (f32x4){snd[4 * g], snd[4 * g + 1], snd[4 * g + 2], snd[4 * g + 3]};
            WAIT_LGKM0(); RAW_BAR(); SBAR();
            lds_ptr rx = lds + (wid ^ 4) * 4096 + lane * 16;
            f32x16 keep = half ? y1 : y0;
#pragma unroll
            for (int g = 0; g < 4; ++g) {
                const f32x4 t = *(const LAS f32x4*)(rx + g * 1024);
#pragma unroll
                for (int i = 0; i < 4; ++i) keep[4 * g + i] += t[i];
            }
            char* yg = ws + WS_Y + (size_t)ch * 4096 + (r + 32 * half) * 64 + hh * 16;
            *(u32x4*)(yg) = rows8(keep, 0);
            *(u32x4*)(yg + 32) = rows8(keep, 2);
        }
    }
    TS(1, 3);

    bf16x8 ones;
    { const int row = lane & 15, g = lane >> 4; const short v = (row == (g & 1)) ? (short)0x3f80 : (short)0;
#pragma unroll
      for (int i = 0; i < 8; ++i) ones[i] = v; }
    lds_ptr lp = ring + lane * 16;
    bf16x8 k0 = lds_rd128(lp), k1 = lds_rd128(lp + 1024);
    f32x16 sA = MFMA32(k0, q0, (f32x16){}); sA = MFMA32(k1, q1, sA);
    f32x16 negm;
    {
        float mx = sA[0];
#pragma unroll
        for (int i = 1; i < 16; ++i) mx = fmaxf(mx, sA[i]);
        auto rr = __builtin_amdgcn_permlane32_swap(__float_as_uint(mx), __float_as_uint(mx), false, false);
        mx = fmaxf(__uint_as_float(rr[0]), __uint_as_float(rr[1]));
#pragma unroll
        for (int i = 0; i < 16; ++i) { negm[i] = -mx; sA[i] -= mx; }
    }
    k0 = lds_rd128(lp + 4096); k1 = lds_rd128(lp + 4096 + 1024);
    bf16x8 v0 = lds_rd128(lp + 2048), v1 = (bf16x8){};
    f32x16 o = (f32x16){}, sB;
    f32x4 lsum = (f32x4){};
    u32x4 pP = (u32x4){0u, 0u, 0u, 0u};

#define EX(SC, i) SC[i] = __builtin_amdgcn_exp2f(SC[i])
#define ASTEP(SC, SN, kp, vc, vn, DOQK, DOK, DOV) do {                                           \
        u32x4 p0;                                                                                  \
        if (DOQK) SN = MFMA32(k0, q0, negm);                                                       \
        SBAR(); if (DOK) k0 = lds_rd128(kp);                                                       \
        SBAR(); EX(SC, 0); EX(SC, 1); EX(SC, 2); SBAR();                                           \
        if (DOQK) SN = MFMA32(k1, q1, SN);                                                         \
        SBAR(); if (DOK) k1 = lds_rd128((kp) + 1024);                                              \
        SBAR(); EX(SC, 3); EX(SC, 4); EX(SC, 5); SBAR();                                           \
        o = MFMA32(v1, BC8(pP), o);                                                                \
        SBAR(); v1 = lds_rd128((vc) + 3072);                                                       \
        SBAR(); EX(SC, 6); EX(SC, 7); SBAR();                                                      \
        p0[0] = cvtpk(SC[0], SC[1]); p0[1] = cvtpk(SC[2], SC[3]); SBAR();                          \
        lsum = MFMA16(ones, BC8(pP), lsum);                                                        \
        SBAR(); p0[2] = cvtpk(SC[4], SC[5]); p0[3] = cvtpk(SC[6], SC[7]); SBAR();                  \
        EX(SC, 8); EX(SC, 9); SBAR();                                                              \
        o = MFMA32(v0, BC8(p0), o);                                                                \
        SBAR(); if (DOV) v0 = lds_rd128((vn) + 2048);                                              \
        SBAR(); EX(SC, 10); EX(SC, 11); EX(SC, 12); SBAR();                                        \
        lsum = MFMA16(ones, BC8(p0), lsum);                                                        \
        SBAR(); EX(SC, 13); EX(SC, 14); EX(SC, 15); SBAR();                                        \
        pP[0] = cvtpk(SC[8], SC[9]); pP[1] = cvtpk(SC[10], SC[11]);                                \
        pP[2] = cvtpk(SC[12], SC[13]); pP[3] = cvtpk(SC[14], SC[15]); SBAR();                      \
    } while (0)

    int slot = 0;
    for (int j = 0; j < 15; ++j) {
        lds_ptr c = lp + slot * AT_STAGE;
        const int nslot = (slot == 2) ? 0 : slot + 1;
        lds_ptr n = lp + nslot * AT_STAGE;
        ASTEP(sA, sB, c + 2 * 4096, c, c + 4096, true, true, true);
        ASTEP(sB, sA, c + 3 * 4096, c + 4096, c + 2 * 4096, true, true, true);
        WAIT_VM(0); RAW_BAR(); SBAR();
        if (j + 2 < 16) issue(j + 2, (nslot == 2) ? 0 : nslot + 1);
        SBAR();
        ASTEP(sA, sB, n, c + 2 * 4096, c + 3 * 4096, true, true, true);
        ASTEP(sB, sA, n + 4096, c + 3 * 4096, n, true, true, true);
        slot = nslot;
    }
    {
        lds_ptr c = lp + slot * AT_STAGE;
        ASTEP(sA, sB, c + 2 * 4096, c, c + 4096, true, true, true);
        ASTEP(sB, sA, c + 3 * 4096, c + 4096, c + 2 * 4096, true, true, true);
        ASTEP(sA, sB, c, c + 2 * 4096, c + 3 * 4096, true, false, true);
        ASTEP(sB, sA, c, c + 3 * 4096, c, false, false, false);
        o = MFMA32(v1, BC8(pP), o);
        lsum = MFMA16(ones, BC8(pP), lsum);
    }
    TS(1, 4);
    const float la = __shfl(lsum[0], r & 15), lb = __shfl(lsum[1], r & 15);
    float l = (r < 16) ? la : lb;
    bool bad = !(l < 1.0e30f);
#pragma unroll
    for (int i = 0; i < 16; ++i) bad |= !(fabsf(o[i]) < 1.0e37f);
    if (__any(bad)) attn_safe(kvg, q0, q1, lane, o, l);
    const float rl = 1.0f / l;
    char* og = ws + WS_O + (size_t)(T * 32 + head) * 2048 + lane * 16;
    *(u32x4*)(og) = pack8(o, 0, rl);
    *(u32x4*)(og + 1024) = pack8(o, 8, rl);
    TS(1, 5);
}

constexpr int G2_LDS = 131072;

__global__ __launch_bounds__(512, 2) void k_back(const char* __restrict__ aq, const char* __restrict__ ws, const char* __restrict__ wo, char* __restrict__ out) {
    extern __shared__ __attribute__((aligned(16))) char smem[];
    lds_ptr lds = (lds_ptr)smem;
    const int tid = threadIdx.x, lane = tid & 63;
    const int wid = __builtin_amdgcn_readfirstlane(tid >> 6);
    const int xcd = blockIdx.x & 7, idx = blockIdx.x >> 3;
    const int mt = 4 * (xcd & 3) + (idx & 3), nt = 8 * (xcd >> 2) + (idx >> 2);
    TS(2, 0);
    const int tg = wid & 3, kh = wid >> 2;
    const int T = 4 * mt + tg;
    const int r = lane & 31, hh = lane >> 5;
    const rsrc_t wr = mk_rsrc(wo, 1024 * 2048);
    const unsigned wv = ((lane >> 2) & 7) * 2048 + nt * 128 + (lane >> 5) * 64 + (lane & 3) * 16;
    const char* og = ws + WS_O + (size_t)T * 65536 + kh * 32768 + lane * 16;
    bf16x8 of[32];
#pragma unroll
    for (int c = 0; c < 4; ++c) {
#pragma unroll
        for (int i = 0; i < 4; ++i) {
            const int kb = 64 * (i >> 1) + 16 * c + 2 * tg + 8 * kh + (i & 1);
            bdma16(wr, wv, kb * 8 * 2048, lds + kb * 1024);
        }
#pragma unroll
        for (int i = 0; i < 8; ++i) of[8 * c + i] = *(const bf16x8*)(og + (8 * c + i) * 1024);
    }
    SBAR();
    ssm_epi(aq, ws, out, 2 * xcd + (idx & 1), idx >> 1, tid);
    SBAR();
    lds_ptr wa = lds + kh * 65536 + (4 * hh + ((lane >> 2) & 3)) * 64 + ((lane >> 4) & 1) * 32 + (lane & 3) * 8;
    f32x16 acc0 = (f32x16){}, acc1 = (f32x16){};
    WAIT_VM(0);
    RAW_BAR(); SBAR();
    TS(2, 1);
#pragma unroll
    for (int kk = 0; kk < 32; ++kk) {
        lds_ptr b = wa + kk * 2048;
        const bf16x8 w0 = cat4(lds_tr(b), lds_tr(b + 1024));
        const bf16x8 w1 = cat4(lds_tr(b + 512), lds_tr(b + 1024 + 512));
        acc0 = MFMA32(w0, of[kk], acc0);
        acc1 = MFMA32(w1, of[kk], acc1);
    }
    TS(2, 2);
    RAW_BAR(); SBAR();
    {
        lds_ptr ex = lds + wid * 4096 + lane * 16;
        const f32x16& snd = kh ? acc0 : acc1;
#pragma unroll
        for (int g = 0; g < 4; ++g) *(LAS f32x4*)(ex + g * 1024) = (f32x4){snd[4 * g], snd[4 * g + 1], snd[4 * g + 2], snd[4 * g + 3]};
        WAIT_LGKM0(); RAW_BAR(); SBAR();
        lds_ptr rx = lds + (wid ^ 4) * 4096 + lane * 16;
        f32x16 keep = kh ? acc1 : acc0;
#pragma unroll
        for (int g = 0; g < 4; ++g) {
            const f32x4 t = *(const LAS f32x4*)(rx + g * 1024);
#pragma unroll
            for (int i = 0; i < 4; ++i) keep[4 * g + i] += t[i];
        }
        char* orow = out + (size_t)(32 * T + r) * 4096 + 2048 + nt * 128 + kh * 64 + hh * 16;
        *(u32x4*)(orow) = rows8(keep, 0);
        *(u32x4*)(orow + 32) = rows8(keep, 2);
    }
    TS(2, 3);
}

extern "C" void kernel_launch(void* const* d_in, const int* in_sizes, int n_in, void* d_out, int out_size, void* d_ws, size_t ws_size,
                              hipStream_t stream) {
    const char* A_q = (const char*)d_in[0];
    const char* A_k = (const char*)d_in[1];
    const char* A_v = (const char*)d_in[2];
    const unsigned short* A_shk = (const unsigned short*)d_in[3];
    const unsigned short* A_ssmk = (const unsigned short*)d_in[4];
    const unsigned short* A_ssmd = (const unsigned short*)d_in[5];
    const char* B_x = (const char*)d_in[6];
    const char* B_wqkv = (const char*)d_in[7];
    const char* B_wo = (const char*)d_in[8];
    char* ws = (char*)d_ws;
    char* out = (char*)d_out;
    (void)hipFuncSetAttribute((const void*)k_front, hipFuncAttributeMaxDynamicSharedMemorySize, G1_LDS);
    (void)hipFuncSetAttribute((const void*)k_mid, hipFuncAttributeMaxDynamicSharedMemorySize, MID_LDS);
    (void)hipFuncSetAttribute((const void*)k_back, hipFuncAttributeMaxDynamicSharedMemorySize, G2_LDS);
    k_front<<<256, 512, G1_LDS, stream>>>(B_x, B_wqkv, A_k, A_v, A_shk, ws);
    k_mid<<<256, 512, MID_LDS, stream>>>(A_ssmk, A_ssmd, ws);
    k_back<<<256, 512, G2_LDS, stream>>>(A_q, ws, B_wo, out);
}
```

```cpp
#include <hip/hip_runtime.h>
#include <hip/hip_bf16.h>
#include <stdint.h>

typedef short bf16x8 __attribute__((ext_vector_type(8)));
typedef short s16x4 __attribute__((ext_vector_type(4)));
typedef float f32x16 __attribute__((ext_vector_type(16)));
typedef float f32x4 __attribute__((ext_vector_type(4)));
typedef float f32x2 __attribute__((ext_vector_type(2)));
typedef __bf16 bf16x2n __attribute__((ext_vector_type(2)));
typedef unsigned u32x4 __attribute__((ext_vector_type(4)));
typedef unsigned u32x2 __attribute__((ext_vector_type(2)));
typedef short v4i16_t __attribute__((ext_vector_type(4)));
#define LAS __attribute__((address_space(3)))
typedef LAS char* lds_ptr;

#define SBAR() __builtin_amdgcn_sched_barrier(0)
#define MFMA32(a, b, c) __builtin_amdgcn_mfma_f32_32x32x16_bf16(a, b, c, 0, 0, 0)
#define MFMA16(a, b, c) __builtin_amdgcn_mfma_f32_16x16x32_bf16(a, b, c, 0, 0, 0)
#define WAIT_VM(n) asm volatile("s_waitcnt vmcnt(" #n ")" ::: "memory")
#define WAIT_LGKM0() asm volatile("s_waitcnt lgkmcnt(0)" ::: "memory")
#define RAW_BAR() asm volatile("s_barrier" ::: "memory")
#define BC8(x) __builtin_bit_cast(bf16x8, x)

#define TS(kid, slot) do {} while (0)

constexpr float QSCALE = 0.17677669529663687f * 1.4426950408889634f;

constexpr unsigned WS_Q = 0;
constexpr unsigned WS_KV = 4u << 20;
constexpr unsigned WS_O = 12u << 20;
constexpr unsigned WS_X = 16u << 20;
constexpr unsigned WS_Y = 20u << 20;
constexpr unsigned WS_FLAG = 24u << 20;
constexpr unsigned FL_X = 0, FL_Q = 8192, FL_Y = 16384, FL_O = 24576, FL_EPOCH = 32768;
constexpr unsigned WS_BYTES = WS_FLAG + 65536;
constexpr int AUX_SC1 = 16;

#define GAS __attribute__((address_space(1)))
typedef GAS unsigned gu32;

__device__ __forceinline__ unsigned cvtpk(float lo, float hi) {
    return __builtin_bit_cast(unsigned, __builtin_convertvector((f32x2){lo, hi}, bf16x2n));
}
__device__ __forceinline__ float bf_lo(unsigned w) { return __uint_as_float(w << 16); }
__device__ __forceinline__ float bf_hi(unsigned w) { return __uint_as_float(w & 0xffff0000u); }
typedef __amdgpu_buffer_rsrc_t rsrc_t;
__device__ __forceinline__ rsrc_t mk_rsrc(const void* p, unsigned bytes) { return __builtin_amdgcn_make_buffer_rsrc((void*)p, 0, (int)bytes, 0x00020000); }
__device__ __forceinline__ void bdma16(rsrc_t r, unsigned voff, unsigned soff, lds_ptr l) {
    __builtin_amdgcn_raw_ptr_buffer_load_lds(r, (LAS void*)l, 16, (int)voff, (int)soff, 0, 0);
}
__device__ __forceinline__ void l2_touch(rsrc_t r, unsigned voff, unsigned soff, lds_ptr junk) {
    __builtin_amdgcn_raw_ptr_buffer_load_lds(r, (LAS void*)junk, 4, (int)voff, (int)soff, 0, 0);
}
__device__ __forceinline__ u32x4 ld16_dev(rsrc_t r, unsigned voff, unsigned soff) {
    return __builtin_bit_cast(u32x4, __builtin_amdgcn_raw_buffer_load_b128(r, (int)voff, (int)soff, AUX_SC1));
}
__device__ __forceinline__ void st16_dev(u32x4 v, rsrc_t r, unsigned voff, unsigned soff) {
    __builtin_amdgcn_raw_buffer_store_b128(v, r, (int)(voff + soff), 0, AUX_SC1);
}
__device__ __forceinline__ void flag_set(char* ws, unsigned off, unsigned epoch) {
    __hip_atomic_store((gu32*)(ws + WS_FLAG + off), epoch, __ATOMIC_RELAXED, __HIP_MEMORY_SCOPE_AGENT);
}
__device__ __forceinline__ void flag_wait(char* ws, unsigned off, int n, unsigned epoch, int lane) {
    gu32* f = (gu32*)(ws + WS_FLAG + off) + (lane < n ? lane : 0);
    for (unsigned spins = 0; spins < (1u << 22); ++spins) {
        const unsigned v = __hip_atomic_load(f, __ATOMIC_RELAXED, __HIP_MEMORY_SCOPE_AGENT);
        if (__all(v == epoch)) break;
        __builtin_amdgcn_s_sleep(2);
    }
    asm volatile("" ::: "memory");
}
__device__ __forceinline__ bf16x8 lds_rd128(lds_ptr p) { return *(const LAS bf16x8*)p; }
__device__ __forceinline__ s16x4 lds_rd64(lds_ptr p) { return *(const LAS s16x4*)p; }
__device__ __forceinline__ s16x4 lds_tr(lds_ptr p) {
    return __builtin_bit_cast(s16x4, __builtin_amdgcn_ds_read_tr16_b64_v4i16((LAS v4i16_t*)p));
}
__device__ __forceinline__ bf16x8 cat4(s16x4 a, s16x4 b) { return (bf16x8){a[0], a[1], a[2], a[3], b[0], b[1], b[2], b[3]}; }
__device__ __forceinline__ u32x4 pack8(const f32x16& v, int o, float sc) {
    return (u32x4){cvtpk(v[o] * sc, v[o + 1] * sc), cvtpk(v[o + 2] * sc, v[o + 3] * sc), cvtpk(v[o + 4] * sc, v[o + 5] * sc), cvtpk(v[o + 6] * sc, v[o + 7] * sc)};
}
__device__ __forceinline__ u32x4 rows8(const f32x16& a, int g) {
    unsigned ax = cvtpk(a[4 * g], a[4 * g + 1]), ay = cvtpk(a[4 * g + 2], a[4 * g + 3]);
    unsigned bx = cvtpk(a[4 * g + 4], a[4 * g + 5]), by = cvtpk(a[4 * g + 6], a[4 * g + 7]);
    auto r0 = __builtin_amdgcn_permlane32_swap(ax, bx, false, false);
    auto r1 = __builtin_amdgcn_permlane32_swap(ay, by, false, false);
    return (u32x4){r0[0], r1[0], r0[1], r1[1]};
}

constexpr int G1_STAGE = 40960, G1_WOFF = 16384;
constexpr int PREP_OFF = 122880, PREP_PITCH = 272;
constexpr int CV_HCOPY = 4160;
constexpr int CV_HCH = 4 * CV_HCOPY;
constexpr int CV_X = 122880;
constexpr int CV_XCH = 95 * 80;
constexpr int CV_EX = 40960;
constexpr int AT_STAGE = 16384;
constexpr int WO_OFF = 49152;
constexpr int JUNK = 163584;
constexpr int ALL_LDS = 163840;
__device__ __forceinline__ constexpr int cv_hoff(int c) { return (c < 2) ? 40960 + c * CV_HCH : 81920 + (c - 2) * CV_HCH; }

__device__ __forceinline__ void attn_safe(const char* __restrict__ kvg, bf16x8 q0, bf16x8 q1, int lane, f32x16& o_out, float& l_out) {
    f32x16 o = (f32x16){};
    float m = -INFINITY, l = 0.f;
    for (int t = 0; t < 64; ++t) {
        const char* b = kvg + (size_t)t * 4096 + lane * 16;
        const bf16x8 k0 = *(const bf16x8*)(b), k1 = *(const bf16x8*)(b + 1024), v0 = *(const bf16x8*)(b + 2048), v1 = *(const bf16x8*)(b + 3072);
        f32x16 s = MFMA32(k0, q0, (f32x16){});
        s = MFMA32(k1, q1, s);
        float mx = s[0];
#pragma unroll
        for (int i = 1; i < 16; ++i) mx = fmaxf(mx, s[i]);
        mx = fmaxf(mx, __shfl_xor(mx, 32));
        const float mn = fmaxf(m, mx);
        const float alpha = (mn == -INFINITY) ? 1.f : __builtin_amdgcn_exp2f(m - mn);
        const float sub = (mn == -INFINITY) ? 0.f : mn;
        m = mn;
        float ps = 0.f;
#pragma unroll
        for (int i = 0; i < 16; ++i) { s[i] = __builtin_amdgcn_exp2f(s[i] - sub); }
        const u32x4 p0 = pack8(s, 0, 1.f), p1 = pack8(s, 8, 1.f);
#pragma unroll
        for (int i = 0; i < 4; ++i) ps += bf_lo(p0[i]) + bf_hi(p0[i]) + bf_lo(p1[i]) + bf_hi(p1[i]);
        l = l * alpha + ps;
#pragma unroll
        for (int i = 0; i < 16; ++i) o[i] *= alpha;
        o = MFMA32(v0, BC8(p0), o);
        o = MFMA32(v1, BC8(p1), o);
    }
    l += __shfl_xor(l, 32);
    o_out = o; l_out = l;
}

struct Args {
    const char* aq; const char* ak; const char* av; const char* shk; const char* ssmk; const char* ssmd;
    const char* x; const char* wqkv; const char* wo; char* out; char* ws;
};

__global__ __launch_bounds__(512, 2) void k_all(Args a) {
    extern __shared__ __attribute__((aligned(16))) char smem[];
    lds_ptr lds = (lds_ptr)smem;
    const int tid = threadIdx.x, lane = tid & 63;
    const int wid = __builtin_amdgcn_readfirstlane(tid >> 6);
    const int xcd = blockIdx.x & 7, idx = blockIdx.x >> 3;
    const int r = lane & 31, hh = lane >> 5;
    char* ws = a.ws;
    const rsrc_t wsr = mk_rsrc(ws, WS_BYTES);
    TS(0, 0);
    const unsigned epoch = __hip_atomic_load((gu32*)(ws + WS_FLAG + FL_EPOCH), __ATOMIC_RELAXED, __HIP_MEMORY_SCOPE_AGENT) + 1u;

    const int mt = 8 * (xcd & 1) + (idx & 7), p = 4 * (xcd >> 1) + (idx >> 3);
    const int cg = 2 * xcd + (idx & 1), tr = idx >> 1;
    const int ch0 = 128 * xcd + 4 * idx;
    const int head = 4 * xcd + (idx >> 3), qt = idx & 7;
    const int mt2 = 4 * (xcd & 3) + (idx & 3), nt = 8 * (xcd >> 2) + (idx >> 2);

    const rsrc_t xr = mk_rsrc(a.x, 2048 * 2048), wr = mk_rsrc(a.wqkv, 1024 * 6144);
    const int pw = wid - 4;
    unsigned xv0 = 0, xv1 = 0, wv = 0;
    auto g1_issue = [&](int kt, int slot) {
        lds_ptr sb = lds + slot * G1_STAGE;
        bdma16(xr, xv0, kt * 128, sb + (4 * pw + 0) * 1024);
        bdma16(xr, xv1, kt * 128 + 8 * 2048, sb + (4 * pw + 1) * 1024);
        bdma16(xr, xv0, kt * 128 + 16 * 2048, sb + (4 * pw + 2) * 1024);
        bdma16(xr, xv1, kt * 128 + 24 * 2048, sb + (4 * pw + 3) * 1024);
#pragma unroll
        for (int kb = 0; kb < 2; ++kb)
#pragma unroll
            for (int seg = 0; seg < 3; ++seg)
                bdma16(wr, wv, kt * 64 * 6144 + kb * 8 * 6144 + seg * 2048, sb + G1_WOFF + ((2 * pw + kb) * 3 + seg) * 1024);
    };
    if (wid >= 4) {
        const int rl = lane >> 3, c = lane & 7;
        xv0 = (128 * mt + 32 * pw + rl) * 2048 + ((c ^ (lane >> 4)) << 4);
        xv1 = (128 * mt + 32 * pw + rl) * 2048 + ((c ^ (4 + (lane >> 4))) << 4);
        wv = (16 * pw + ((lane >> 2) & 7)) * 6144 + p * 128 + (lane >> 5) * 64 + (lane & 3) * 16;
        g1_issue(0, 0); g1_issue(1, 1); g1_issue(2, 2);
    }
    l2_touch(xr, (128 * mt + 32 * (p & 3) + r) * 2048 + hh * 128, wid * 256, lds + JUNK);
    if (wid < 6) { const int s = wid * 64 + lane, row = s / 3, seg = s - 3 * row; l2_touch(wr, (8 * row + (mt & 7)) * 6144 + seg * 2048 + p * 128, 0, lds + JUNK); }
    {
        const int cp = tid & 31, tg = tid >> 5;
        const int ch = 64 * cg + 2 * cp, t0 = 128 * tr + 8 * tg;
        unsigned kr[11], vr[8];
#pragma unroll
        for (int i = 0; i < 11; ++i) {
            const int t = t0 - 3 + i;
            const unsigned x = *(const unsigned*)(a.ak + (size_t)(t < 0 ? 0 : t) * 2048 + ch * 2);
            kr[i] = (i < 3 && t < 0) ? 0u : x;
        }
#pragma unroll
        for (int i = 0; i < 8; ++i) vr[i] = *(const unsigned*)(a.av + (size_t)(t0 + i) * 2048 + ch * 2);
        const u32x4 sw = *(const u32x4*)(a.shk + ch * 8);
#pragma unroll
        for (int c = 0; c < 2; ++c) {
            const float w0 = bf_lo(sw[2 * c]), w1 = bf_hi(sw[2 * c]), w2 = bf_lo(sw[2 * c + 1]), w3 = bf_hi(sw[2 * c + 1]);
            float kf[11], kvf[8];
#pragma unroll
            for (int i = 0; i < 11; ++i) kf[i] = c ? bf_hi(kr[i]) : bf_lo(kr[i]);
#pragma unroll
            for (int i = 0; i < 8; ++i) {
                const float s = kf[i] * w3 + kf[i + 1] * w2 + kf[i + 2] * w1 + kf[i + 3] * w0;
                kvf[i] = s * (c ? bf_hi(vr[i]) : bf_lo(vr[i]));
            }
            const u32x4 rev = (u32x4){cvtpk(kvf[7], kvf[6]), cvtpk(kvf[5], kvf[4]), cvtpk(kvf[3], kvf[2]), cvtpk(kvf[1], kvf[0])};
            *(LAS u32x4*)(lds + PREP_OFF + (2 * cp + c) * PREP_PITCH + tg * 16) = rev;
        }
        WAIT_LGKM0(); RAW_BAR();
#pragma unroll
        for (int i = 0; i < 2; ++i) {
            const int c = 32 * i + (tid >> 4), pc = tid & 15;
            const u32x4 v = *(const LAS u32x4*)(lds + PREP_OFF + c * PREP_PITCH + pc * 16);
            st16_dev(v, wsr, WS_X + (64 * cg + c) * 4096 + tr * 256 + pc * 16, 0);
        }
    }
    WAIT_VM(0); RAW_BAR();
    if (tid == 0) flag_set(ws, FL_X + cg * 256 + tr * 4, epoch);

    if (wid >= 4) {
        const rsrc_t hr = mk_rsrc(a.ssmk, 1024 * 4096);
        u32x4 xin[4];
        unsigned e[8];
        const int ptid = tid - 256;
        const int ec = lane / 5, ew = lane - 5 * ec, ev = (ew < 4) ? ew : 259;
        int slot = 0;
#pragma unroll
        for (int j = 1; j < 16; ++j) {
            if (j == 7 || j == 8) { WAIT_VM(22); } else if (j == 15) { WAIT_VM(8); } else { WAIT_VM(10); }
            RAW_BAR();
            if (j == 6) flag_wait(ws, FL_X + (ch0 >> 6) * 256, 16, epoch, lane);
            if (j + 2 < 16) g1_issue(j + 2, slot);
            slot = (slot == 2) ? 0 : slot + 1;
            if (j == 1) {
                if (pw < 2) l2_touch(hr, ch0 * 4096 + (pw * 64 + lane) * 128, 0, lds + JUNK);
                else l2_touch(mk_rsrc(a.aq, 2048 * 2048), (128 * tr + (pw - 2) * 64 + lane) * 2048 + cg * 128, 0, lds + JUNK);
                l2_touch(mk_rsrc(a.wo, 1024 * 2048), (32 * idx + 8 * pw + (lane >> 3)) * 2048 + (xcd >> 2) * 1024 + (lane & 7) * 128, 0, lds + JUNK);
            }
            if (j == 6) {
                SBAR();
#pragma unroll
                for (int c = 0; c < 4; ++c) xin[c] = ld16_dev(wsr, WS_X + (ch0 + c) * 4096 + ptid * 16, 0);
#pragma unroll
                for (int q = 0; q < 8; ++q) {
                    const int i = 8 * ev + q - 31 + pw, ic = i < 0 ? 0 : (i > 2047 ? 2047 : i);
                    e[q] = __builtin_amdgcn_raw_buffer_load_b16(hr, (ch0 + (ec & 3)) * 4096 + ic * 2, 0, 0);
                }
                SBAR();
            }
            if (j == 9) {
#pragma unroll
                for (int c = 0; c < 4; ++c) *(LAS u32x4*)(lds + CV_X + c * CV_XCH + ((ptid >> 2) + 31) * 80 + (ptid & 3) * 16) = xin[c];
                for (int i = ptid; i < 4 * 155; i += 256) { const int c = i / 155, o = i - 155 * c; *(LAS u32x4*)(lds + CV_X + c * CV_XCH + o * 16) = (u32x4){0u, 0u, 0u, 0u}; }
            }
            if (j >= 14) {
#pragma unroll
                for (int c2 = 0; c2 < 2; ++c2) {
                    const int c = 2 * (j - 14) + c2;
                    const unsigned hv = (ch0 + c) * 4096 + 2 * (8 * (4 + lane) - 31 + pw);
#pragma unroll
                    for (int pc = 0; pc < 4; ++pc) bdma16(hr, hv, pc * 1024, lds + cv_hoff(c) + pw * CV_HCOPY + 64 + pc * 1024);
                }
            }
        }
        WAIT_VM(0);
        if (lane < 20) {
#pragma unroll
            for (int q = 0; q < 8; ++q) { const int i = 8 * ev + q - 31 + pw; e[q] = (i >= 0 && i < 2048) ? (e[q] & 0xffffu) : 0u; }
            const u32x4 edge = (u32x4){e[0] | (e[1] << 16), e[2] | (e[3] << 16), e[4] | (e[5] << 16), e[6] | (e[7] << 16)};
            const int ho = (ec < 2) ? 40960 + ec * CV_HCH : 81920 + (ec - 2) * CV_HCH;
            *(LAS u32x4*)(lds + ho + pw * CV_HCOPY + ev * 16) = edge;
        }
        WAIT_LGKM0();
    } else {
        const int wm = wid >> 1, wn = wid & 1;
        int xa[4];
#pragma unroll
        for (int ks = 0; ks < 4; ++ks) xa[ks] = (64 * wm + r) * 128 + (((2 * ks + hh) ^ ((r >> 1) & 7)) << 4);
        const int wa = G1_WOFF + hh * 3072 + wn * 512 + ((lane >> 2) & 3) * 64 + ((lane >> 4) & 1) * 32 + (lane & 3) * 8;
        f32x16 acc[2][3];
#pragma unroll
        for (int i = 0; i < 2; ++i)
#pragma unroll
            for (int g = 0; g < 3; ++g) acc[i][g] = (f32x16){};
        bf16x8 xf[2][2];
        s16x4 wl[2][3], wh[2][3];
#define G1_STEP(C, N, SB, KS, BARRIER, LOAD) do {                                                                  \
        acc[0][0] = MFMA32(cat4(wl[C][0], wh[C][0]), xf[C][0], acc[0][0]); SBAR();                                   \
        acc[0][1] = MFMA32(cat4(wl[C][1], wh[C][1]), xf[C][0], acc[0][1]); SBAR();                                   \
        if (BARRIER) { WAIT_LGKM0(); RAW_BAR(); SBAR(); }                                                            \
        acc[0][2] = MFMA32(xf[C][0], cat4(wl[C][2], wh[C][2]), acc[0][2]); SBAR();                                   \
        if (LOAD) { wl[N][0] = lds_tr((SB) + wa + (KS) * 6144); wh[N][0] = lds_tr((SB) + wa + (KS) * 6144 + 256);    \
                    xf[N][0] = lds_rd128((SB) + xa[KS]); } SBAR();                                                   \
        acc[1][0] = MFMA32(cat4(wl[C][0], wh[C][0]), xf[C][1], acc[1][0]); SBAR();                                   \
        if (LOAD) { wl[N][1] = lds_tr((SB) + wa + (KS) * 6144 + 1024); wh[N][1] = lds_tr((SB) + wa + (KS) * 6144 + 1280); \
                    wl[N][2] = lds_tr((SB) + wa + (KS) * 6144 + 2048); } SBAR();                                     \
        acc[1][1] = MFMA32(cat4(wl[C][1], wh[C][1]), xf[C][1], acc[1][1]); SBAR();                                   \
        if (LOAD) { wh[N][2] = lds_tr((SB) + wa + (KS) * 6144 + 2304); xf[N][1] = lds_rd128((SB) + xa[KS] + 4096); } SBAR(); \
        acc[1][2] = MFMA32(xf[C][1], cat4(wl[C][2], wh[C][2]), acc[1][2]); SBAR();                                   \
    } while (0)
        SBAR();
        TS(0, 1);
        xf[0][0] = lds_rd128(lds + xa[0]); xf[0][1] = lds_rd128(lds + xa[0] + 4096);
#pragma unroll
        for (int g = 0; g < 3; ++g) { wl[0][g] = lds_tr(lds + wa + g * 1024); wh[0][g] = lds_tr(lds + wa + g * 1024 + 256); }
        SBAR();
        int slot = 0;
        for (int kt = 0; kt < 15; ++kt) {
            lds_ptr sb = lds + slot * G1_STAGE;
            slot = (slot == 2) ? 0 : slot + 1;
            lds_ptr sn = lds + slot * G1_STAGE;
            G1_STEP(0, 1, sb, 1, false, true);
            G1_STEP(1, 0, sb, 2, false, true);
            G1_STEP(0, 1, sb, 3, false, true);
            G1_STEP(1, 0, sn, 0, true, true);
        }
        {
            lds_ptr sb = lds + slot * G1_STAGE;
            G1_STEP(0, 1, sb, 1, false, true);
            G1_STEP(1, 0, sb, 2, false, true);
            G1_STEP(0, 1, sb, 3, false, true);
            G1_STEP(1, 0, sb, 0, false, false);
        }
        TS(0, 2);
        const int hd = 2 * p + wn;
#pragma unroll
        for (int tt = 0; tt < 2; ++tt) {
            const int T = 4 * mt + 2 * wm + tt;
            const unsigned q = WS_Q + (hd * 64 + T) * 2048, kv = WS_KV + (hd * 64 + T) * 4096;
            st16_dev(pack8(acc[tt][0], 0, QSCALE), wsr, lane * 16, q);
            st16_dev(pack8(acc[tt][0], 8, QSCALE), wsr, lane * 16, q + 1024);
            st16_dev(pack8(acc[tt][1], 0, 1.f), wsr, lane * 16, kv);
            st16_dev(pack8(acc[tt][1], 8, 1.f), wsr, lane * 16, kv + 1024);
            st16_dev(pack8(acc[tt][2], 0, 1.f), wsr, lane * 16, kv + 2048);
            st16_dev(pack8(acc[tt][2], 8, 1.f), wsr, lane * 16, kv + 3072);
        }
        WAIT_VM(0);
    }
    RAW_BAR(); SBAR();
    if (tid == 0) flag_set(ws, FL_Q + p * 256 + mt * 4, epoch);
    TS(0, 3);

    const char* kvg = ws + WS_KV + (size_t)head * 262144;
    const rsrc_t kvr = mk_rsrc(kvg, 262144);
    const unsigned kvo = wid * 2048 + lane * 16;
    auto at_issue = [&](int st, int slot) {
        bdma16(kvr, kvo, st * AT_STAGE, lds + slot * AT_STAGE + wid * 2048);
        bdma16(kvr, kvo + 1024, st * AT_STAGE, lds + slot * AT_STAGE + wid * 2048 + 1024);
    };
    const int T = 8 * qt + wid;
    bf16x8 q0, q1;
    {
        const int cc = wid & 3, half = wid >> 2;
        const int ch = ch0 + cc;
        const unsigned dbits = ((const unsigned short*)a.ssmd)[ch];
        f32x16 y0 = (f32x16){}, y1 = (f32x16){};
        lds_ptr ha = lds + ((cc < 2) ? 40960 + cc * CV_HCH : 81920 + (cc - 2) * CV_HCH) + (r & 3) * CV_HCOPY + 48 - 16 * hh + 8 * (r >> 2) + half * 1024;
        lds_ptr hc = ha + 8; asm volatile("" : "+v"(hc));
        lds_ptr xb = lds + CV_X + cc * CV_XCH + (r + 31 - 16 * half) * 80 + hh * 16;
        if (half == 0) {
            bf16x8 dk[2];
#pragma unroll
            for (int ks = 0; ks < 2; ++ks) {
                const int j = 16 * ks + 8 * hh + 7 - r;
                u32x4 d;
#pragma unroll
                for (int jj = 0; jj < 4; ++jj) d[jj] = (j == 2 * jj) ? dbits : ((j == 2 * jj + 1) ? (dbits << 16) : 0u);
                dk[ks] = BC8(d);
            }
            y0 = MFMA32(dk[0], lds_rd128(xb), y0); y1 = MFMA32(dk[0], lds_rd128(xb + 2560), y1);
            y0 = MFMA32(dk[1], lds_rd128(xb + 32), y0); y1 = MFMA32(dk[1], lds_rd128(xb + 2560 + 32), y1);
        }
        s16x4 al[2][4], ah[2][4];
        bf16x8 bb[2][4];
#define CV_LDA(N, i, D) do { al[N][i] = lds_rd64(ha + 32 * (D)); ah[N][i] = lds_rd64(hc + 32 * (D)); } while (0)
#define CV_STEP(C, N, M1, LOAD) do {                                                                               \
        y0 = MFMA32(cat4(al[C][0], ah[C][0]), bb[C][0], y0); SBAR();                                                 \
        if (LOAD) { CV_LDA(N, 0, 2 * (M1)); } SBAR();                                                                \
        y1 = MFMA32(cat4(al[C][0], ah[C][0]), bb[C][2], y1); SBAR();                                                 \
        if (LOAD) { bb[N][0] = lds_rd128(xb - 80 * (M1)); bb[N][2] = lds_rd128(xb - 80 * (M1) + 2560); } SBAR();     \
        y0 = MFMA32(cat4(al[C][1], ah[C][1]), bb[C][1], y0); SBAR();                                                 \
        if (LOAD) { CV_LDA(N, 1, 2 * (M1) - 1); } SBAR();                                                            \
        y1 = MFMA32(cat4(al[C][1], ah[C][1]), bb[C][3], y1); SBAR();                                                 \
        if (LOAD) { bb[N][1] = lds_rd128(xb - 80 * (M1) + 32); bb[N][3] = lds_rd128(xb - 80 * (M1) + 2560 + 32); } SBAR(); \
        y1 = MFMA32(cat4(al[C][2], ah[C][2]), bb[C][0], y1); SBAR();                                                 \
        if (LOAD) { CV_LDA(N, 2, 2 * (M1) + 64); } SBAR();                                                           \
        y1 = MFMA32(cat4(al[C][3], ah[C][3]), bb[C][1], y1); SBAR();                                                 \
        if (LOAD) { CV_LDA(N, 3, 2 * (M1) + 63); } SBAR();                                                           \
    } while (0)
        CV_LDA(0, 0, 0); bb[0][0] = lds_rd128(xb); bb[0][2] = lds_rd128(xb + 2560);
        CV_LDA(0, 1, -1); bb[0][1] = lds_rd128(xb + 32); bb[0][3] = lds_rd128(xb + 2560 + 32);
        CV_LDA(0, 2, 64); CV_LDA(0, 3, 63);
        SBAR();
#pragma unroll
        for (int m = 0; m < 16; m += 2) {
            if (m == 8 && wid == 0) {
                flag_wait(ws, FL_Q + (head >> 1) * 256, 16, epoch, lane);
                asm volatile("buffer_inv sc1" ::: "memory");
                SBAR();
            }
            CV_STEP(0, 1, m + 1, true);
            CV_STEP(1, 0, m + 2, (m + 2 < 16));
        }
        TS(0, 4);
        WAIT_VM(0);
        RAW_BAR(); SBAR();
        at_issue(0, 0); at_issue(1, 1);
        {
            const char* qg = ws + WS_Q + (size_t)(head * 64 + T) * 2048 + lane * 16;
            q0 = *(const bf16x8*)qg; q1 = *(const bf16x8*)(qg + 1024);
        }
        lds_ptr ex = lds + CV_EX + wid * 4096 + lane * 16;
        const f32x16& snd = half ? y0 : y1;
#pragma unroll
        for (int g = 0; g < 4; ++g) *(LAS f32x4*)(ex + g * 1024) = (f32x4){snd[4 * g], snd[4 * g + 1], snd[4 * g + 2], snd[4 * g + 3]};
        WAIT_LGKM0(); RAW_BAR(); SBAR();
        lds_ptr rx = lds + CV_EX + (wid ^ 4) * 4096 + lane * 16;
        f32x16 keep = half ? y1 : y0;
#pragma unroll
        for (int g = 0; g < 4; ++g) {
            const f32x4 t = *(const LAS f32x4*)(rx + g * 1024);
#pragma unroll
            for (int i = 0; i < 4; ++i) keep[4 * g + i] += t[i];
        }
        const unsigned yg = WS_Y + ch * 4096 + (r + 32 * half) * 64 + hh * 16;
        st16_dev(rows8(keep, 0), wsr, yg, 0);
        st16_dev(rows8(keep, 2), wsr, yg + 32, 0);
    }
    TS(0, 5);

    const rsrc_t wor = mk_rsrc(a.wo, 1024 * 2048);
    const unsigned wov = ((lane >> 2) & 7) * 2048 + nt * 128 + (lane >> 5) * 64 + (lane & 3) * 16;
    f32x16 o = (f32x16){};
    float l;
    {
        bf16x8 ones;
        { const int row = lane & 15, g = lane >> 4; const short v = (row == (g & 1)) ? (short)0x3f80 : (short)0;
#pragma unroll
          for (int i = 0; i < 8; ++i) ones[i] = v; }
        lds_ptr lp = lds + lane * 16;
        WAIT_VM(4); RAW_BAR(); SBAR();
        bf16x8 k0 = lds_rd128(lp), k1 = lds_rd128(lp + 1024);
        f32x16 sA = MFMA32(k0, q0, (f32x16){}); sA = MFMA32(k1, q1, sA);
        f32x16 negm;
        {
            float mx = sA[0];
#pragma unroll
            for (int i = 1; i < 16; ++i) mx = fmaxf(mx, sA[i]);
            auto rr = __builtin_amdgcn_permlane32_swap(__float_as_uint(mx), __float_as_uint(mx), false, false);
            mx = fmaxf(__uint_as_float(rr[0]), __uint_as_float(rr[1]));
#pragma unroll
            for (int i = 0; i < 16; ++i) { negm[i] = -mx; sA[i] -= mx; }
        }
        k0 = lds_rd128(lp + 4096); k1 = lds_rd128(lp + 4096 + 1024);
        bf16x8 v0 = lds_rd128(lp + 2048), v1 = (bf16x8){};
        f32x16 sB;
        f32x4 lsum = (f32x4){};
        u32x4 pP = (u32x4){0u, 0u, 0u, 0u};

#define EX(SC, i) SC[i] = __builtin_amdgcn_exp2f(SC[i])
#define ASTEP(SC, SN, kp, vc, vn, DOQK, DOK, DOV) do {                                           \
        u32x4 p0;                                                                                  \
        if (DOQK) SN = MFMA32(k0, q0, negm);                                                       \
        SBAR(); if (DOK) k0 = lds_rd128(kp);                                                       \
        SBAR(); EX(SC, 0); EX(SC, 1); EX(SC, 2); SBAR();                                           \
        if (DOQK) SN = MFMA32(k1, q1, SN);                                                         \
        SBAR(); if (DOK) k1 = lds_rd128((kp) + 1024);                                              \
        SBAR(); EX(SC, 3); EX(SC, 4); EX(SC, 5); SBAR();                                           \
        o = MFMA32(v1, BC8(pP), o);                                                                \
        SBAR(); v1 = lds_rd128((vc) + 3072);                                                       \
        SBAR(); EX(SC, 6); EX(SC, 7); SBAR();                                                      \
        p0[0] = cvtpk(SC[0], SC[1]); p0[1] = cvtpk(SC[2], SC[3]); SBAR();                          \
        lsum = MFMA16(ones, BC8(pP), lsum);                                                        \
        SBAR(); p0[2] = cvtpk(SC[4], SC[5]); p0[3] = cvtpk(SC[6], SC[7]); SBAR();                  \
        EX(SC, 8); EX(SC, 9); SBAR();                                                              \
        o = MFMA32(v0, BC8(p0), o);                                                                \
        SBAR(); if (DOV) v0 = lds_rd128((vn) + 2048);                                              \
        SBAR(); EX(SC, 10); EX(SC, 11); EX(SC, 12); SBAR();                                        \
        lsum = MFMA16(ones, BC8(p0), lsum);                                                        \
        SBAR(); EX(SC, 13); EX(SC, 14); EX(SC, 15); SBAR();                                        \
        pP[0] = cvtpk(SC[8], SC[9]); pP[1] = cvtpk(SC[10], SC[11]);                                \
        pP[2] = cvtpk(SC[12], SC[13]); pP[3] = cvtpk(SC[14], SC[15]); SBAR();                      \
    } while (0)

        int slot = 0;
        for (int j = 0; j < 15; ++j) {
            lds_ptr c = lp + slot * AT_STAGE;
            const int nslot = (slot == 2) ? 0 : slot + 1;
            lds_ptr n = lp + nslot * AT_STAGE;
            ASTEP(sA, sB, c + 2 * 4096, c, c + 4096, true, true, true);
            ASTEP(sB, sA, c + 3 * 4096, c + 4096, c + 2 * 4096, true, true, true);
            if (j == 14 && wid == 0) flag_wait(ws, FL_Y + cg * 256, 16, epoch, lane);
            if (j == 0) { WAIT_VM(0); } else { WAIT_VM(1); }
            RAW_BAR(); SBAR();
            if (j == 0 && tid == 0) flag_set(ws, FL_Y + (ch0 >> 6) * 256 + ((ch0 >> 2) & 15) * 4, epoch);
            if (j + 2 < 16) at_issue(j + 2, (nslot == 2) ? 0 : nslot + 1);
            if (j < 14) { const int kb = 8 * j + wid; bdma16(wor, wov, kb * 8 * 2048, lds + WO_OFF + kb * 1024); }
            SBAR();
            ASTEP(sA, sB, n, c + 2 * 4096, c + 3 * 4096, true, true, true);
            ASTEP(sB, sA, n + 4096, c + 3 * 4096, n, true, true, true);
            slot = nslot;
        }
        {
            lds_ptr c = lp + slot * AT_STAGE;
            ASTEP(sA, sB, c + 2 * 4096, c, c + 4096, true, true, true);
            ASTEP(sB, sA, c + 3 * 4096, c + 4096, c + 2 * 4096, true, true, true);
            ASTEP(sA, sB, c, c + 2 * 4096, c + 3 * 4096, true, false, true);
            ASTEP(sB, sA, c, c + 3 * 4096, c, false, false, false);
            o = MFMA32(v1, BC8(pP), o);
            lsum = MFMA16(ones, BC8(pP), lsum);
        }
        TS(0, 6);
        const float la = __shfl(lsum[0], r & 15), lb = __shfl(lsum[1], r & 15);
        l = (r < 16) ? la : lb;
    }
    const int ecp = tid & 31, etg = tid >> 5;
    const int ech = 64 * cg + 2 * ecp, et0 = 128 * tr + 8 * etg;
    const u32x4 ey0 = ld16_dev(wsr, WS_Y + ech * 4096 + et0 * 2, 0), ey1 = ld16_dev(wsr, WS_Y + (ech + 1) * 4096 + et0 * 2, 0);
    unsigned qr[8];
#pragma unroll
    for (int i = 0; i < 8; ++i) qr[i] = *(const unsigned*)(a.aq + (size_t)(et0 + i) * 2048 + ech * 2);
    {
        bool bad = !(l < 1.0e30f);
#pragma unroll
        for (int i = 0; i < 16; ++i) bad |= !(fabsf(o[i]) < 1.0e37f);
        if (__any(bad)) attn_safe(kvg, q0, q1, lane, o, l);
        const float rl = 1.0f / l;
        const unsigned og = WS_O + (T * 32 + head) * 2048;
        st16_dev(pack8(o, 0, rl), wsr, lane * 16, og);
        st16_dev(pack8(o, 8, rl), wsr, lane * 16, og + 1024);
    }
    WAIT_VM(0); RAW_BAR(); SBAR();
    if (tid == 0) flag_set(ws, FL_O + qt * 256 + head * 4, epoch);
#pragma unroll
    for (int i = 0; i < 2; ++i) { const int kb = 112 + 2 * wid + i; bdma16(wor, wov, kb * 8 * 2048, lds + (kb - 112) * 1024); }
#pragma unroll
    for (int i = 0; i < 8; ++i) {
        const float ya = (i & 1) ? bf_hi(ey0[i >> 1]) : bf_lo(ey0[i >> 1]);
        const float yb = (i & 1) ? bf_hi(ey1[i >> 1]) : bf_lo(ey1[i >> 1]);
        *(unsigned*)(a.out + (size_t)(et0 + i) * 4096 + ech * 2) = cvtpk(bf_lo(qr[i]) * ya, bf_hi(qr[i]) * yb);
    }
    TS(0, 7);

    {
        const int tg = wid & 3, kh = wid >> 2;
        const int T2 = 4 * mt2 + tg;
        if (wid == 0) flag_wait(ws, FL_O + (mt2 >> 1) * 256, 32, epoch, lane);
        WAIT_VM(0); RAW_BAR(); SBAR();
        if (blockIdx.x == 0 && tid == 0) flag_set(ws, FL_EPOCH, epoch);
        bf16x8 of[32];
#pragma unroll
        for (int i = 0; i < 32; ++i) of[i] = BC8(ld16_dev(wsr, lane * 16 + i * 1024, WS_O + T2 * 65536 + kh * 32768));
        const int wl = (4 * hh + ((lane >> 2) & 3)) * 64 + ((lane >> 4) & 1) * 32 + (lane & 3) * 8;
        lds_ptr wa = lds + WO_OFF + kh * 65536 + wl;
        lds_ptr wb = kh ? (lds + wl - 24 * 2048) : wa;
        f32x16 acc0 = (f32x16){}, acc1 = (f32x16){};
#pragma unroll
        for (int kk = 0; kk < 32; ++kk) {
            lds_ptr b = (kk < 24 ? wa : wb) + kk * 2048;
            const bf16x8 w0 = cat4(lds_tr(b), lds_tr(b + 1024));
            const bf16x8 w1 = cat4(lds_tr(b + 512), lds_tr(b + 1024 + 512));
            acc0 = MFMA32(w0, of[kk], acc0);
            acc1 = MFMA32(w1, of[kk], acc1);
        }
        RAW_BAR(); SBAR();
        lds_ptr ex = lds + WO_OFF + wid * 4096 + lane * 16;
        const f32x16& snd = kh ? acc0 : acc1;
#pragma unroll
        for (int g = 0; g < 4; ++g) *(LAS f32x4*)(ex + g * 1024) = (f32x4){snd[4 * g], snd[4 * g + 1], snd[4 * g + 2], snd[4 * g + 3]};
        WAIT_LGKM0(); RAW_BAR(); SBAR();
        lds_ptr rx = lds + WO_OFF + (wid ^ 4) * 4096 + lane * 16;
        f32x16 keep = kh ? acc1 : acc0;
#pragma unroll
        for (int g = 0; g < 4; ++g) {
            const f32x4 t = *(const LAS f32x4*)(rx + g * 1024);
#pragma unroll
            for (int i = 0; i < 4; ++i) keep[4 * g + i] += t[i];
        }
        char* orow = a.out + (size_t)(32 * T2 + r) * 4096 + 2048 + nt * 128 + kh * 64 + hh * 16;
        *(u32x4*)(orow) = rows8(keep, 0);
        *(u32x4*)(orow + 32) = rows8(keep, 2);
    }
}

extern "C" void kernel_launch(void* const* d_in, const int* in_sizes, int n_in, void* d_out, int out_size, void* d_ws, size_t ws_size,
                              hipStream_t stream) {
    Args a;
    a.aq = (const char*)d_in[0]; a.ak = (const char*)d_in[1]; a.av = (const char*)d_in[2];
    a.shk = (const char*)d_in[3]; a.ssmk = (const char*)d_in[4]; a.ssmd = (const char*)d_in[5];
    a.x = (const char*)d_in[6]; a.wqkv = (const char*)d_in[7]; a.wo = (const char*)d_in[8];
    a.out = (char*)d_out; a.ws = (char*)d_ws;
    (void)hipFuncSetAttribute((const void*)k_all, hipFuncAttributeMaxDynamicSharedMemorySize, ALL_LDS);
    k_all<<<256, 512, ALL_LDS, stream>>>(a);
}
```

```cpp
#include <hip/hip_runtime.h>
#include <hip/hip_bf16.h>
#include <stdint.h>

typedef short bf16x8 __attribute__((ext_vector_type(8)));
typedef short s16x4 __attribute__((ext_vector_type(4)));
typedef float f32x16 __attribute__((ext_vector_type(16)));
typedef float f32x4 __attribute__((ext_vector_type(4)));
typedef float f32x2 __attribute__((ext_vector_type(2)));
typedef __bf16 bf16x2n __attribute__((ext_vector_type(2)));
typedef unsigned u32x4 __attribute__((ext_vector_type(4)));
typedef unsigned u32x2 __attribute__((ext_vector_type(2)));
typedef short v4i16_t __attribute__((ext_vector_type(4)));
#define LAS __attribute__((address_space(3)))
typedef LAS char* lds_ptr;

#define SBAR() __builtin_amdgcn_sched_barrier(0)
#define MFMA32(a, b, c) __builtin_amdgcn_mfma_f32_32x32x16_bf16(a, b, c, 0, 0, 0)
#define MFMA16(a, b, c) __builtin_amdgcn_mfma_f32_16x16x32_bf16(a, b, c, 0, 0, 0)
#define WAIT_VM(n) asm volatile("s_waitcnt vmcnt(" #n ")" ::: "memory")
#define WAIT_LGKM0() asm volatile("s_waitcnt lgkmcnt(0)" ::: "memory")
#define RAW_BAR() asm volatile("s_barrier" ::: "memory")
#define BC8(x) __builtin_bit_cast(bf16x8, x)

constexpr float QSCALE = 0.17677669529663687f * 1.4426950408889634f;

constexpr unsigned WS_Q = 0;
constexpr unsigned WS_KV = 4u << 20;
constexpr unsigned WS_O = 12u << 20;
constexpr unsigned WS_X = 16u << 20;
constexpr unsigned WS_Y = 20u << 20;
constexpr unsigned WS_FLAG = 24u << 20;
constexpr unsigned FL_X = 0, FL_Q = 8192, FL_Y = 16384, FL_O = 24576, FL_EPOCH = 32768;
constexpr unsigned WS_BYTES = WS_FLAG + 65536;
constexpr int AUX_SC1 = 16;

#define GAS __attribute__((address_space(1)))
typedef GAS unsigned gu32;

__device__ __forceinline__ unsigned cvtpk(float lo, float hi) {
    return __builtin_bit_cast(unsigned, __builtin_convertvector((f32x2){lo, hi}, bf16x2n));
}
__device__ __forceinline__ float bf_lo(unsigned w) { return __uint_as_float(w << 16); }
__device__ __forceinline__ float bf_hi(unsigned w) { return __uint_as_float(w & 0xffff0000u); }
typedef __amdgpu_buffer_rsrc_t rsrc_t;
__device__ __forceinline__ rsrc_t mk_rsrc(const void* p, unsigned bytes) { return __builtin_amdgcn_make_buffer_rsrc((void*)p, 0, (int)bytes, 0x00020000); }
__device__ __forceinline__ void bdma16(rsrc_t r, unsigned voff, unsigned soff, lds_ptr l) {
    __builtin_amdgcn_raw_ptr_buffer_load_lds(r, (LAS void*)l, 16, (int)voff, (int)soff, 0, 0);
}
__device__ __forceinline__ void bdma16_dev(rsrc_t r, unsigned voff, unsigned soff, lds_ptr l) {
    __builtin_amdgcn_raw_ptr_buffer_load_lds(r, (LAS void*)l, 16, (int)voff, (int)soff, 0, AUX_SC1);
}
__device__ __forceinline__ void l2_touch(rsrc_t r, unsigned voff, unsigned soff, lds_ptr junk) {
    __builtin_amdgcn_raw_ptr_buffer_load_lds(r, (LAS void*)junk, 4, (int)voff, (int)soff, 0, 0);
}
__device__ __forceinline__ void l2_touch_raw(const void* base, unsigned bytes, unsigned voff, unsigned junk) {
    const unsigned long long b = (unsigned long long)base;
    const u32x4 r = (u32x4){(unsigned)b, (unsigned)(b >> 32) & 0xffffu, bytes, 0x00020000u};
    asm volatile("s_mov_b32 m0, %2\n\ts_nop 4\n\tbuffer_load_dword %0, %1, 0 offen lds" :: "v"(voff), "s"(r), "s"(junk) : "m0");
}
__device__ __forceinline__ u32x4 ld16_dev(rsrc_t r, unsigned voff, unsigned soff) {
    return __builtin_bit_cast(u32x4, __builtin_amdgcn_raw_buffer_load_b128(r, (int)voff, (int)soff, AUX_SC1));
}
__device__ __forceinline__ void st16_dev(u32x4 v, rsrc_t r, unsigned voff, unsigned soff) {
    __builtin_amdgcn_raw_buffer_store_b128(v, r, (int)(voff + soff), 0, AUX_SC1);
}
__device__ __forceinline__ void flag_set(char* ws, unsigned off, unsigned epoch) {
    __hip_atomic_store((gu32*)(ws + WS_FLAG + off), epoch, __ATOMIC_RELAXED, __HIP_MEMORY_SCOPE_AGENT);
}
__device__ __forceinline__ void flag_wait(char* ws, unsigned off, int n, int st, unsigned epoch, int lane) {
    gu32* f = (gu32*)(ws + WS_FLAG + off + (lane < n ? lane : 0) * st);
    unsigned v0 = __hip_atomic_load(f, __ATOMIC_RELAXED, __HIP_MEMORY_SCOPE_AGENT);
#pragma unroll 1
    for (unsigned spins = 0; spins < (1u << 22); ++spins) {
        __builtin_amdgcn_s_sleep(4);
        const unsigned v1 = __hip_atomic_load(f, __ATOMIC_RELAXED, __HIP_MEMORY_SCOPE_AGENT);
        if (__all(v0 == epoch)) break;
        __builtin_amdgcn_s_sleep(4);
        v0 = __hip_atomic_load(f, __ATOMIC_RELAXED, __HIP_MEMORY_SCOPE_AGENT);
        if (__all(v1 == epoch)) break;
    }
    asm volatile("" ::: "memory");
}
__device__ __forceinline__ unsigned flag_peek(char* ws, unsigned off, int n, int lane) {
    return __hip_atomic_load((gu32*)(ws + WS_FLAG + off) + (lane < n ? lane : 0), __ATOMIC_RELAXED, __HIP_MEMORY_SCOPE_AGENT);
}
__device__ __forceinline__ bf16x8 lds_rd128(lds_ptr p) { return *(const LAS bf16x8*)p; }
__device__ __forceinline__ s16x4 lds_rd64(lds_ptr p) { return *(const LAS s16x4*)p; }
__device__ __forceinline__ s16x4 lds_tr(lds_ptr p) {
    return __builtin_bit_cast(s16x4, __builtin_amdgcn_ds_read_tr16_b64_v4i16((LAS v4i16_t*)p));
}
__device__ __forceinline__ bf16x8 cat4(s16x4 a, s16x4 b) { return (bf16x8){a[0], a[1], a[2], a[3], b[0], b[1], b[2], b[3]}; }
__device__ __forceinline__ u32x4 pack8(const f32x16& v, int o, float sc) {
    return (u32x4){cvtpk(v[o] * sc, v[o + 1] * sc), cvtpk(v[o + 2] * sc, v[o + 3] * sc), cvtpk(v[o + 4] * sc, v[o + 5] * sc), cvtpk(v[o + 6] * sc, v[o + 7] * sc)};
}
__device__ __forceinline__ float mul1(float a, float b) { float r; asm("v_mul_f32_e32 %0, %1, %2" : "=v"(r) : "v"(a), "v"(b)); return r; }
__device__ __forceinline__ float add1(float a, float b) { float r; asm("v_add_f32_e32 %0, %1, %2" : "=v"(r) : "v"(a), "v"(b)); return r; }
__device__ __forceinline__ u32x4 pack8m(const f32x16& v, int o, float sc) {
    return (u32x4){cvtpk(mul1(v[o], sc), mul1(v[o + 1], sc)), cvtpk(mul1(v[o + 2], sc), mul1(v[o + 3], sc)),
                   cvtpk(mul1(v[o + 4], sc), mul1(v[o + 5], sc)), cvtpk(mul1(v[o + 6], sc), mul1(v[o + 7], sc))};
}
__device__ __forceinline__ u32x4 rows8(const f32x16& a, int g) {
    unsigned ax = cvtpk(a[4 * g], a[4 * g + 1]), ay = cvtpk(a[4 * g + 2], a[4 * g + 3]);
    unsigned bx = cvtpk(a[4 * g + 4], a[4 * g + 5]), by = cvtpk(a[4 * g + 6], a[4 * g + 7]);
    auto r0 = __builtin_amdgcn_permlane32_swap(ax, bx, false, false);
    auto r1 = __builtin_amdgcn_permlane32_swap(ay, by, false, false);
    return (u32x4){r0[0], r1[0], r0[1], r1[1]};
}

constexpr int G1_STAGE = 40960, G1_WOFF = 16384;
constexpr int PREP_OFF = 122880, PREP_PITCH = 272;
constexpr int CV_HCOPY = 4160;
constexpr int CV_HCH = 2 * CV_HCOPY;
constexpr int CV_H = 48640;
constexpr int CV_H2 = 4 * CV_HCH;
constexpr int CV_X = 122880;
constexpr int CV_XCH = 95 * 64;
constexpr int AT_STAGE = 16384;
constexpr int WO_OFF = 49152;
constexpr int WO_TAIL = 16384;
constexpr int JUNK = 163584;
constexpr int SYNC = 160000;
constexpr int ALL_LDS = 163840;
#define BC0_M 12
#define CV_PRIO 2
#define OUTA_AUX 0
#define PK 4
#define KVC 0
#define KVH 2
#define JX 12
#define JPEEK 11
#define OUTB_AUX 0
#define EP_EARLY 1
#define EP_SLEEP 7
#define EP_SLEEP2 10
#define G2_D 16
#define PT_OUT 3
#define AT_PRIO 0
#define LOOP_PRIO 0
#define CODE_TOUCH 27008

constexpr int blk_after(int j) {
    return (j == PK + 1 ? 4 : 0) + ((j == 1 || j == 3 || j == 7) ? 2 : 0) + (j == KVH ? 20 : 0) + (j == JPEEK ? 1 : 0) + (j == 11 ? 5 : 0) + (j == JX ? 4 : 0) + (j == 14 ? 8 : 0);
}
constexpr int blk_all(int j) { return (j == PK + 4 ? 1 : 0) + (j + 2 < 16 ? 10 : 0) + blk_after(j); }
constexpr int blk_wait(int j) {
    return j == 1 ? 10 : (j == 2 ? blk_all(1) : blk_after(j - 2) + blk_all(j - 1));
}
template <int N> __device__ __forceinline__ void wait_vm_c() { asm volatile("s_waitcnt vmcnt(%0)" :: "n"(N) : "memory"); }
template <int J> __device__ __forceinline__ void wait_stage(int j) {
    if constexpr (J < 16) { if (j == J) wait_vm_c<blk_wait(J)>(); else wait_stage<J + 1>(j); }
}

__device__ __forceinline__ void attn_safe(const char* __restrict__ kvg, bf16x8 q0, bf16x8 q1, int lane, f32x16& o_out, float& l_out) {
    f32x16 o = (f32x16){};
    float m = -INFINITY, l = 0.f;
    for (int t = 0; t < 64; ++t) {
        const char* b = kvg + (size_t)t * 4096 + lane * 16;
        const bf16x8 k0 = *(const bf16x8*)(b), k1 = *(const bf16x8*)(b + 1024), v0 = *(const bf16x8*)(b + 2048), v1 = *(const bf16x8*)(b + 3072);
        f32x16 s = MFMA32(k0, q0, (f32x16){});
        s = MFMA32(k1, q1, s);
        float mx = s[0];
#pragma unroll
        for (int i = 1; i < 16; ++i) mx = fmaxf(mx, s[i]);
        mx = fmaxf(mx, __shfl_xor(mx, 32));
        const float mn = fmaxf(m, mx);
        const float alpha = (mn == -INFINITY) ? 1.f : __builtin_amdgcn_exp2f(m - mn);
        const float sub = (mn == -INFINITY) ? 0.f : mn;
        m = mn;
        float ps = 0.f;
#pragma unroll
        for (int i = 0; i < 16; ++i) { s[i] = __builtin_amdgcn_exp2f(s[i] - sub); }
        const u32x4 p0 = pack8(s, 0, 1.f), p1 = pack8(s, 8, 1.f);
#pragma unroll
        for (int i = 0; i < 4; ++i) ps += bf_lo(p0[i]) + bf_hi(p0[i]) + bf_lo(p1[i]) + bf_hi(p1[i]);
        l = l * alpha + ps;
#pragma unroll
        for (int i = 0; i < 16; ++i) o[i] *= alpha;
        o = MFMA32(v0, BC8(p0), o);
        o = MFMA32(v1, BC8(p1), o);
    }
    l += __shfl_xor(l, 32);
    o_out = o; l_out = l;
}

struct Args {
    const char* aq; const char* ak; const char* av; const char* shk; const char* ssmk; const char* ssmd;
    const char* x; const char* wqkv; const char* wo; char* out; char* ws;
};

__global__ __launch_bounds__(512, 2) void k_all(Args a) {
    extern __shared__ __attribute__((aligned(16))) char smem[];
    lds_ptr lds = (lds_ptr)smem;
    const int tid = threadIdx.x, lane = tid & 63;
    const int wid = __builtin_amdgcn_readfirstlane(tid >> 6);
    const int xcd = blockIdx.x & 7, idx = blockIdx.x >> 3;
    const int r = lane & 31, hh = lane >> 5;
    if (wid < 4) l2_touch_raw((const void*)(__builtin_amdgcn_s_getpc() & ~127ull), CODE_TOUCH, tid * 128, JUNK);
    char* ws = a.ws;
    const rsrc_t wsr = mk_rsrc(ws, WS_BYTES);
    unsigned epoch = __hip_atomic_load((gu32*)(ws + WS_FLAG + FL_EPOCH), __ATOMIC_RELAXED, __HIP_MEMORY_SCOPE_AGENT);

    const int mt = 8 * (xcd & 1) + (idx & 7), p = 4 * (xcd >> 1) + (idx >> 3);
    const int cg = 2 * xcd + (idx & 1), tr = idx >> 1;
    const int ch0 = 128 * xcd + 4 * idx;
    const int head = 4 * xcd + (idx >> 3), qt = idx & 7;
    const int mt2 = 4 * (xcd & 3) + (idx & 3), nt = 8 * (xcd >> 2) + (idx >> 2);
    const int pw = wid & 3;

    const int cp = tid & 31, tg = tid >> 5;
    unsigned kr[11], vr[8];
    u32x4 sw;
    auto kvload = [&]() {
        const int ch = 64 * cg + 2 * cp, t0 = 128 * tr + 8 * tg;
        const rsrc_t kr_ = mk_rsrc(a.ak, 2048 * 2048), vr_ = mk_rsrc(a.av, 2048 * 2048);
        const unsigned vb = t0 * 2048 + ch * 2, hb = (t0 == 0) ? vb : vb - 3 * 2048;
#pragma unroll
        for (int i = 0; i < 3; ++i) kr[i] = __builtin_amdgcn_raw_buffer_load_b32(kr_, hb, i * 2048, 0);
#pragma unroll
        for (int i = 3; i < 11; ++i) kr[i] = __builtin_amdgcn_raw_buffer_load_b32(kr_, vb, (i - 3) * 2048, 0);
#pragma unroll
        for (int i = 0; i < 8; ++i) vr[i] = __builtin_amdgcn_raw_buffer_load_b32(vr_, vb, i * 2048, 0);
        sw = *(const u32x4*)(a.shk + ch * 8);
    };
    auto prep = [&]() {
        const bool first = (128 * tr + 8 * tg) == 0;
#pragma unroll
        for (int c = 0; c < 2; ++c) {
            const float w0 = bf_lo(sw[2 * c]), w1 = bf_hi(sw[2 * c]), w2 = bf_lo(sw[2 * c + 1]), w3 = bf_hi(sw[2 * c + 1]);
            float kf[11], kvf[8];
#pragma unroll
            for (int i = 0; i < 11; ++i) { kf[i] = c ? bf_hi(kr[i]) : bf_lo(kr[i]); if (i < 3 && first) kf[i] = 0.f; }
#pragma unroll
            for (int i = 0; i < 8; ++i) {
                const float s = kf[i] * w3 + kf[i + 1] * w2 + kf[i + 2] * w1 + kf[i + 3] * w0;
                kvf[i] = s * (c ? bf_hi(vr[i]) : bf_lo(vr[i]));
            }
            const u32x4 rev = (u32x4){cvtpk(kvf[7], kvf[6]), cvtpk(kvf[5], kvf[4]), cvtpk(kvf[3], kvf[2]), cvtpk(kvf[1], kvf[0])};
            *(LAS u32x4*)(lds + PREP_OFF + (2 * cp + c) * PREP_PITCH + tg * 16) = rev;
        }
    };
    const rsrc_t wor = mk_rsrc(a.wo, 1024 * 2048);
    const unsigned wov = ((lane >> 2) & 7) * 2048 + nt * 128 + (lane >> 5) * 64 + (lane & 3) * 16;

    u32x4 oa[4];
    unsigned oaddr = 0;
    const rsrc_t outr = mk_rsrc(a.out, 2048 * 4096);
    auto oa_store = [&]() {
#pragma unroll
        for (int i = 0; i < 4; ++i) __builtin_amdgcn_raw_buffer_store_b128(oa[i], outr, oaddr + i * 4096, 0, OUTA_AUX);
    };
    if (wid >= 4) {
        const rsrc_t xr = mk_rsrc(a.x, 2048 * 2048), wr = mk_rsrc(a.wqkv, 1024 * 6144);
        const int rl = lane >> 3, c8 = lane & 7;
        const unsigned xv0 = (128 * mt + 32 * pw + rl) * 2048 + ((c8 ^ (lane >> 4)) << 4);
        const unsigned xv1 = (128 * mt + 32 * pw + rl) * 2048 + ((c8 ^ (4 + (lane >> 4))) << 4);
        const unsigned wv = (16 * pw + ((lane >> 2) & 7)) * 6144 + p * 128 + (lane >> 5) * 64 + (lane & 3) * 16;
        auto g1_issue = [&](int kt, int slot) {
            lds_ptr sb = lds + slot * G1_STAGE;
            bdma16(xr, xv0, kt * 128, sb + (4 * pw + 0) * 1024);
            bdma16(xr, xv1, kt * 128 + 8 * 2048, sb + (4 * pw + 1) * 1024);
            bdma16(xr, xv0, kt * 128 + 16 * 2048, sb + (4 * pw + 2) * 1024);
            bdma16(xr, xv1, kt * 128 + 24 * 2048, sb + (4 * pw + 3) * 1024);
#pragma unroll
            for (int kb = 0; kb < 2; ++kb)
#pragma unroll
                for (int seg = 0; seg < 3; ++seg)
                    bdma16(wr, wv, kt * 64 * 6144 + kb * 8 * 6144 + seg * 2048, sb + G1_WOFF + ((2 * pw + kb) * 3 + seg) * 1024);
        };
        const unsigned tx = (128 * mt + 32 * (p & 3) + r) * 2048, tw = (8 * (mt & 7) + (lane % 24) / 3) * 6144 + ((lane % 24) % 3) * 2048 + p * 128;
        auto g1_touch = [&](int st) {
            l2_touch(xr, tx, st * 128, lds + JUNK);
            l2_touch(wr, tw, st * 64 * 6144, lds + JUNK);
        };
        const int ch = ch0 + pw;
        const unsigned dbits = ((const unsigned short*)a.ssmd)[ch];
        SBAR();
        g1_touch(pw);
        g1_issue(0, 0); g1_issue(1, 1); g1_issue(2, 2);
        SBAR();
        WAIT_VM(20);
        RAW_BAR(); SBAR();
        asm volatile("" : "+v"(epoch));
        epoch += 1u;
        const int ptid = tid - 256;
        u32x4 hg[5], hc0[5], hc2[5], hh1, hh3;
        unsigned fx = 0;
        const unsigned flx = FL_X + (ch0 >> 6) * 256 + ((ch0 >> 2) & 3) * 64;
        int slot = 0;
#pragma unroll
        for (int j = 1; j < 16; ++j) {
            if (j == PK + 1) WAIT_LGKM0();
            wait_stage<1>(j);
            RAW_BAR();
            SBAR();
            u32x4 xv[4];
            if (j == PK + 1) {
#pragma unroll
                for (int i = 0; i < 4; ++i) xv[i] = *(const LAS u32x4*)(lds + PREP_OFF + (16 * i + (ptid >> 4)) * PREP_PITCH + (ptid & 15) * 16);
                SBAR();
            }
            if (j == PK + 4 && lane == 0) flag_set(ws, FL_X + cg * 256 + pw * 64 + tr * 4, epoch);
            SBAR();
            if (j + 2 < 16) g1_issue(j + 2, slot);
            slot = (slot == 2) ? 0 : slot + 1;
            SBAR();
            if (j == PK + 1) {
#pragma unroll
                for (int i = 0; i < 4; ++i) st16_dev(xv[i], wsr, WS_X + (64 * cg + 16 * i + (ptid >> 4)) * 4096 + tr * 256 + (ptid & 15) * 16, 0);
            }
            if (j == 1) g1_touch(4 + pw);
            if (j == 3) g1_touch(8 + pw);
            if (j == PK + 3) {
#pragma unroll
                for (int q = 0; q < 2; ++q)
                    if (lane + 64 * q < 124) *(LAS u32x4*)(lds + CV_X + pw * CV_XCH + (lane + 64 * q) * 16) = (u32x4){0u, 0u, 0u, 0u};
            }
            if (j == 7) g1_touch(12 + pw);
            if (j == KVH) { SBAR(); kvload(); SBAR(); }
            if (j == JPEEK) fx = flag_peek(ws, flx, 16, lane);
            if (j == PK) {
                SBAR();
#pragma unroll
                for (int i = 0; i < 11; ++i) asm volatile("" : "+v"(kr[i]));
#pragma unroll
                for (int i = 0; i < 8; ++i) asm volatile("" : "+v"(vr[i]));
                asm volatile("" : "+v"(sw));
                prep();
            }
            if (j == JX) {
                if (!__all(fx == epoch)) flag_wait(ws, flx, 16, 4, epoch, lane);
                asm volatile("" ::: "memory");
                SBAR();
#pragma unroll
                for (int i = 0; i < 4; ++i)
                    bdma16_dev(wsr, WS_X + ch * 4096 + (lane >> 2) * 64 + (((lane & 3) ^ (((31 + (lane >> 2)) >> 2) & 3)) << 4), i * 1024,
                               lds + CV_X + pw * CV_XCH + 31 * 64 + i * 1024);
            }
            if (j == 11) {
                const rsrc_t hr = mk_rsrc(a.ssmk + ch * 4096, 4096);
#pragma unroll
                for (int i = 0; i < 5; ++i) {
                    const int w = 63 * i + lane - 4;
                    hg[i] = __builtin_bit_cast(u32x4, __builtin_amdgcn_raw_buffer_load_b128(hr, w < 0 ? 0x40000000 : w * 16, 0, 0));
                }
            }
            if (j == 13) {
#pragma unroll
                for (int i = 0; i < 5; ++i) {
                    const unsigned d0 = hg[i][0], d1 = hg[i][1], d2 = hg[i][2], d3 = hg[i][3];
                    const unsigned n0 = __builtin_amdgcn_update_dpp(0u, d0, 0x130, 0xf, 0xf, true), n1 = __builtin_amdgcn_update_dpp(0u, d1, 0x130, 0xf, 0xf, true);
                    const unsigned a01 = __builtin_amdgcn_alignbit(d1, d0, 16), a12 = __builtin_amdgcn_alignbit(d2, d1, 16), a23 = __builtin_amdgcn_alignbit(d3, d2, 16);
                    const unsigned a3n = __builtin_amdgcn_alignbit(n0, d3, 16), ann = __builtin_amdgcn_alignbit(n1, n0, 16);
                    hc0[i] = (u32x4){a01, a12, a23, a3n};
                    hc2[i] = (u32x4){a12, a23, a3n, ann};
                    asm volatile("" : "+v"(hc0[i]), "+v"(hc2[i]));
                    if (i == 0) { hh1 = (u32x4){d1, d2, d3, n0}; hh3 = (u32x4){d2, d3, n0, n1}; asm volatile("" : "+v"(hh1), "+v"(hh3)); }
                }
            }
            if (j == 14) {
                const rsrc_t hr = mk_rsrc(a.ssmk + ch * 4096, 4096);
#pragma unroll
                for (int c = 0; c < 2; ++c)
#pragma unroll
                    for (int i = 0; i < 4; ++i)
                        bdma16(hr, lane * 16 + 4 + 4 * c, i * 1024, lds + CV_H + pw * CV_HCH + c * CV_HCOPY + 64 + i * 1024);
                SBAR();
                if (lane < 4) {
                    lds_ptr hp = lds + CV_H + pw * CV_HCH + lane * 16;
                    *(LAS u32x4*)(hp) = hh1;
                    *(LAS u32x4*)(hp + CV_HCOPY) = hh3;
                }
            }
            SBAR();
        }
#pragma unroll
        for (int i = 0; i < 5; ++i)
            if (lane < (i < 4 ? 63 : 8)) {
                lds_ptr hp = lds + CV_H + CV_H2 + pw * CV_HCH + (63 * i + lane) * 16;
                *(LAS u32x4*)(hp) = hc0[i];
                *(LAS u32x4*)(hp + CV_HCOPY) = hc2[i];
            }
        WAIT_VM(0);
        SBAR();
        f32x16 y0 = (f32x16){}, y1 = (f32x16){};
        {
            lds_ptr ha = lds + CV_H + ((r & 1) ^ 1) * CV_H2 + pw * CV_HCH + ((r >> 1) & 1) * CV_HCOPY + 48 - 16 * hh + 8 * (r >> 2);
            lds_ptr hc = ha + 8; asm volatile("" : "+v"(hc));
            int xc = r + 31;
            const unsigned xbase = (unsigned)(size_t)(lds + CV_X + pw * CV_XCH);
            auto xaddr = [&](int m) { const int row = xc - m; return (lds_ptr)(size_t)(xbase + row * 64 + ((hh ^ ((row >> 2) & 3)) << 4)); };
            auto xalt = [&](lds_ptr p) { return (lds_ptr)(size_t)((unsigned)(size_t)p ^ 32u); };
            {
                bf16x8 dk[2];
#pragma unroll
                for (int ks = 0; ks < 2; ++ks) {
                    const int j = 16 * ks + 8 * hh + 7 - r;
                    u32x4 d;
#pragma unroll
                    for (int jj = 0; jj < 4; ++jj) d[jj] = (j == 2 * jj) ? dbits : ((j == 2 * jj + 1) ? (dbits << 16) : 0u);
                    dk[ks] = BC8(d);
                }
                lds_ptr x0 = xaddr(0), x1 = xalt(x0);
                y0 = MFMA32(dk[0], lds_rd128(x0), y0); y1 = MFMA32(dk[0], lds_rd128(x0 + 2048), y1);
                y0 = MFMA32(dk[1], lds_rd128(x1), y0); y1 = MFMA32(dk[1], lds_rd128(x1 + 2048), y1);
            }
            s16x4 al[2][4], ah[2][4];
            bf16x8 bb[2][4];
#define CV_LDA(N, i, D) do { al[N][i] = lds_rd64(ha + 32 * (D)); ah[N][i] = lds_rd64(hc + 32 * (D)); } while (0)
#define CV_STEP(C, N, M1) do {                                                                                     \
            y0 = MFMA32(cat4(al[C][0], ah[C][0]), bb[C][0], y0); SBAR();                                             \
            CV_LDA(N, 0, 2 * (M1)); SBAR();                                                                          \
            y1 = MFMA32(cat4(al[C][0], ah[C][0]), bb[C][2], y1); SBAR();                                             \
            lds_ptr xa_ = xaddr(M1);                                                                                 \
            bb[N][0] = lds_rd128(xa_); bb[N][2] = lds_rd128(xa_ + 2048); SBAR();                                     \
            y0 = MFMA32(cat4(al[C][1], ah[C][1]), bb[C][1], y0); SBAR();                                             \
            CV_LDA(N, 1, 2 * (M1) - 1); SBAR();                                                                      \
            y1 = MFMA32(cat4(al[C][1], ah[C][1]), bb[C][3], y1); SBAR();                                             \
            xa_ = xalt(xa_);                                                                                         \
            bb[N][1] = lds_rd128(xa_); bb[N][3] = lds_rd128(xa_ + 2048); SBAR();                                     \
            y1 = MFMA32(cat4(al[C][2], ah[C][2]), bb[C][0], y1); SBAR();                                             \
            CV_LDA(N, 2, 2 * (M1) + 64); SBAR();                                                                     \
            y1 = MFMA32(cat4(al[C][3], ah[C][3]), bb[C][1], y1); SBAR();                                             \
            CV_LDA(N, 3, 2 * (M1) + 63); SBAR();                                                                     \
        } while (0)
            CV_LDA(0, 0, 0); bb[0][0] = lds_rd128(xaddr(0)); bb[0][2] = lds_rd128(xaddr(0) + 2048);
            CV_LDA(0, 1, -1); bb[0][1] = lds_rd128(xalt(xaddr(0))); bb[0][3] = lds_rd128(xalt(xaddr(0)) + 2048);
            CV_LDA(0, 2, 64); CV_LDA(0, 3, 63);
            SBAR();
#pragma unroll 1
            for (int h2 = 0; h2 < 2; ++h2) {
#pragma unroll
                for (int m = 0; m < 16; m += 2) {
                    if (m == BC0_M && h2 == 0) { RAW_BAR(); SBAR(); }
                    CV_STEP(0, 1, m + 1);
                    CV_STEP(1, 0, m + 2);
                }
                ha += 1024; hc += 1024; xc -= 16;
                __builtin_amdgcn_s_setprio(CV_PRIO);
            }
            __builtin_amdgcn_s_setprio(AT_PRIO);
        }
        SBAR();
        {
            const rsrc_t kvr = mk_rsrc(ws + WS_KV + (size_t)head * 262144, 262144);
            const unsigned kvo = pw * 4096 + lane * 16;
            const unsigned kvt = lane < 4 ? (qt * 16 + pw * 4 + lane) * 128 : 0x40000000u;
            const rsrc_t aqr = mk_rsrc(a.aq, 2048 * 2048);
            const int ech = 64 * cg + 8 * (ptid & 7), et = 128 * tr + 4 * (ptid >> 3);
            u32x2 ey[8];
            u32x4 eq[4];
            unsigned fy = 0;
            unsigned yb = WS_Y + ech * 4096 + et * 2, qb = et * 2048 + ech * 2;
            asm volatile("" : "+v"(yb), "+v"(qb));
            int slot = 2 * AT_STAGE;
            auto ring = [&](int j, bool touch) {
#pragma unroll
                for (int i = 0; i < 4; ++i) bdma16_dev(kvr, kvo + i * 1024, (j + 2) * AT_STAGE, lds + slot + pw * 4096 + i * 1024);
                slot = (slot == 2 * AT_STAGE) ? 0 : slot + AT_STAGE;
#pragma unroll
                for (int i = 0; i < 2; ++i) { const int kb = 8 * j + 2 * pw + i; bdma16(wor, wov, kb * 8 * 2048, lds + WO_OFF + kb * 1024); }
                if (touch) l2_touch(kvr, kvt, (j + 6) * AT_STAGE, lds + JUNK);
            };
            u32x4 yv[4];
            {
                lds_ptr yw = lds + CV_X + pw * CV_XCH + r * 80 + hh * 16;
                *(LAS u32x4*)(yw) = rows8(y0, 0); *(LAS u32x4*)(yw + 32) = rows8(y0, 2);
                *(LAS u32x4*)(yw + 2560) = rows8(y1, 0); *(LAS u32x4*)(yw + 2560 + 32) = rows8(y1, 2);
#pragma unroll
                for (int i = 0; i < 4; ++i) yv[i] = *(const LAS u32x4*)(lds + CV_X + pw * CV_XCH + (16 * i + (lane >> 2)) * 80 + (lane & 3) * 16);
                unsigned yo = WS_Y + ch * 4096 + lane * 16;
                asm volatile("" : "+v"(yv[0]), "+v"(yv[1]), "+v"(yv[2]), "+v"(yv[3]), "+v"(yo));
                SBAR();
                RAW_BAR(); SBAR();
                ring(0, true);
                SBAR();
#pragma unroll
                for (int i = 0; i < 4; ++i) st16_dev(yv[i], wsr, yo, i * 1024);
            }
            SBAR();
            WAIT_VM(7); RAW_BAR(); SBAR();
            ring(1, true);
            SBAR();
#pragma unroll 1
            for (int j = 2; j < 10; ++j) {
                if (PT_OUT && j > 2) WAIT_VM(4); else WAIT_VM(3);
                RAW_BAR(); SBAR();
                if (j == 2 && lane == 0) flag_set(ws, FL_Y + (ch >> 6) * 256 + (ch & 63) * 4, epoch);
                ring(j, true);
                if (PT_OUT) {
                    unsigned vo = 0x40000000u;
                    if (j == 4 && (PT_OUT & 1) && lane < 32) vo = WS_O + ((8 * qt + 2 * pw + (lane >> 4)) * 32 + head) * 2048 + (lane & 15) * 128;
                    if (j == 4 || j == 2 || j == 3) l2_touch(wsr, vo, 0, lds + JUNK);
                    else {
                        if (j == 6 && (PT_OUT & 2) && lane < 32) vo = (128 * tr + 32 * pw + lane) * 4096 + cg * 128;
                        if (j == 8 && (PT_OUT & 2) && lane < 32) vo = (128 * mt2 + 32 * pw + lane) * 4096 + 2048 + nt * 128;
                        l2_touch(outr, vo, 0, lds + JUNK);
                    }
                }
                SBAR();
            }
#pragma unroll 1
            for (int j = 10; j < 13; ++j) {
                WAIT_VM(2); RAW_BAR(); SBAR();
                ring(j, false);
                SBAR();
            }
            WAIT_VM(2); RAW_BAR(); SBAR();
            ring(13, false);
            auto ep_load = [&]() {
                if (!__all(fy == epoch)) flag_wait(ws, FL_Y + cg * 256, 64, 4, epoch, lane);
                asm volatile("" ::: "memory");
                SBAR();
#pragma unroll
                for (int c = 0; c < 8; ++c)
                    ey[c] = __builtin_bit_cast(u32x2, __builtin_amdgcn_raw_buffer_load_b64(wsr, yb, c * 4096, AUX_SC1));
#pragma unroll
                for (int i = 0; i < 4; ++i)
                    eq[i] = __builtin_bit_cast(u32x4, __builtin_amdgcn_raw_buffer_load_b128(aqr, qb, i * 2048, 0));
                SBAR();
            };
            if (EP_EARLY) fy = flag_peek(ws, FL_Y + cg * 256, 64, lane);
            SBAR();
            if (EP_EARLY) WAIT_VM(3); else WAIT_VM(2);
            RAW_BAR(); SBAR();
#pragma unroll
            for (int i = 0; i < 4; ++i) { const int kb = 112 + 4 * pw + i; bdma16(wor, wov, kb * 8 * 2048, lds + WO_TAIL + (kb - 112) * 1024); }
            if (EP_EARLY) ep_load(); else fy = flag_peek(ws, FL_Y + cg * 256, 64, lane);
            SBAR();
            RAW_BAR(); SBAR();
            __builtin_amdgcn_s_setprio(0);
            if (EP_SLEEP) __builtin_amdgcn_s_sleep(EP_SLEEP);
            SBAR();
            WAIT_VM(0);
            SBAR();
            if (!EP_EARLY) ep_load();
#pragma unroll
            for (int i = 0; i < 4; ++i) {
                u32x4 o;
#pragma unroll
                for (int c = 0; c < 4; ++c) {
                    const float ya = (i & 1) ? bf_hi(ey[2 * c][i >> 1]) : bf_lo(ey[2 * c][i >> 1]);
                    const float yb = (i & 1) ? bf_hi(ey[2 * c + 1][i >> 1]) : bf_lo(ey[2 * c + 1][i >> 1]);
                    o[c] = cvtpk(mul1(bf_lo(eq[i][c]), ya), mul1(bf_hi(eq[i][c]), yb));
                }
                oa[i] = o;
            }
            oaddr = et * 4096 + ech * 2;
            asm volatile("" : "+v"(oa[0]), "+v"(oa[1]), "+v"(oa[2]), "+v"(oa[3]), "+v"(oaddr));
            SBAR();
            RAW_BAR(); SBAR();
            if (EP_SLEEP2) __builtin_amdgcn_s_sleep(EP_SLEEP2);
            SBAR();
            oa_store();
        }
    } else {
        if (tid < 4) *(LAS unsigned*)(lds + SYNC + tid * 4) = 0u;
        WAIT_LGKM0();
        RAW_BAR(); SBAR();
        if (KVC == 0) kvload();
        SBAR();
        const int wm = wid >> 1, wn = wid & 1;
        {
            int xa[4];
#pragma unroll
            for (int ks = 0; ks < 4; ++ks) xa[ks] = (64 * wm + r) * 128 + (((2 * ks + hh) ^ ((r >> 1) & 7)) << 4);
            const int wa = G1_WOFF + hh * 3072 + wn * 512 + ((lane >> 2) & 3) * 64 + ((lane >> 4) & 1) * 32 + (lane & 3) * 8;
            f32x16 acc[2][3];
#pragma unroll
            for (int i = 0; i < 2; ++i)
#pragma unroll
                for (int g = 0; g < 3; ++g) acc[i][g] = (f32x16){};
            bf16x8 xf[2][2];
            s16x4 wl[2][3], wh[2][3];
#define G1_STEP(C, N, SB, KS, BARRIER, LOAD) do {                                                                  \
            acc[0][0] = MFMA32(cat4(wl[C][0], wh[C][0]), xf[C][0], acc[0][0]); SBAR();                               \
            acc[0][1] = MFMA32(cat4(wl[C][1], wh[C][1]), xf[C][0], acc[0][1]); SBAR();                               \
            if (BARRIER) { WAIT_LGKM0(); RAW_BAR(); SBAR(); }                                                        \
            acc[0][2] = MFMA32(xf[C][0], cat4(wl[C][2], wh[C][2]), acc[0][2]); SBAR();                               \
            if (LOAD) { wl[N][0] = lds_tr((SB) + wa + (KS) * 6144); wh[N][0] = lds_tr((SB) + wa + (KS) * 6144 + 256); \
                        xf[N][0] = lds_rd128((SB) + xa[KS]); } SBAR();                                               \
            acc[1][0] = MFMA32(cat4(wl[C][0], wh[C][0]), xf[C][1], acc[1][0]); SBAR();                               \
            if (LOAD) { wl[N][1] = lds_tr((SB) + wa + (KS) * 6144 + 1024); wh[N][1] = lds_tr((SB) + wa + (KS) * 6144 + 1280); \
                        wl[N][2] = lds_tr((SB) + wa + (KS) * 6144 + 2048); } SBAR();                                 \
            acc[1][1] = MFMA32(cat4(wl[C][1], wh[C][1]), xf[C][1], acc[1][1]); SBAR();                               \
            if (LOAD) { wh[N][2] = lds_tr((SB) + wa + (KS) * 6144 + 2304); xf[N][1] = lds_rd128((SB) + xa[KS] + 4096); } SBAR(); \
            acc[1][2] = MFMA32(xf[C][1], cat4(wl[C][2], wh[C][2]), acc[1][2]); SBAR();                               \
        } while (0)
            SBAR();
            xf[0][0] = lds_rd128(lds + xa[0]); xf[0][1] = lds_rd128(lds + xa[0] + 4096);
#pragma unroll
            for (int g = 0; g < 3; ++g) { wl[0][g] = lds_tr(lds + wa + g * 1024); wh[0][g] = lds_tr(lds + wa + g * 1024 + 256); }
            SBAR();
            int slot = 0;
#pragma clang loop unroll(disable)
            for (int kt = 0; kt < 15; ++kt) {
                if (KVC > 0 && kt == KVC) { kvload(); SBAR(); }
                if (kt == PK) {
#pragma unroll
                    for (int i = 0; i < 11; ++i) asm volatile("" : "+v"(kr[i]));
#pragma unroll
                    for (int i = 0; i < 8; ++i) asm volatile("" : "+v"(vr[i]));
                    asm volatile("" : "+v"(sw));
                    prep();
                    SBAR();
                }
                lds_ptr sb = lds + slot * G1_STAGE;
                slot = (slot == 2) ? 0 : slot + 1;
                lds_ptr sn = lds + slot * G1_STAGE;
                G1_STEP(0, 1, sb, 1, false, true);
                G1_STEP(1, 0, sb, 2, false, true);
                G1_STEP(0, 1, sb, 3, false, true);
                G1_STEP(1, 0, sn, 0, true, true);
            }
            __builtin_amdgcn_s_setprio(3);
            asm volatile("" : "+v"(epoch));
            epoch += 1u;
            {
                lds_ptr sb = lds + slot * G1_STAGE;
                G1_STEP(0, 1, sb, 1, false, true);
                G1_STEP(1, 0, sb, 2, false, true);
                G1_STEP(0, 1, sb, 3, false, true);
                G1_STEP(1, 0, sb, 0, false, false);
            }
            const int hd = 2 * p + wn;
#pragma unroll
            for (int tt = 0; tt < 2; ++tt) {
                const int T = 4 * mt + 2 * wm + tt;
                const unsigned kv = WS_KV + (hd * 64 + T) * 4096;
                st16_dev(pack8(acc[tt][1], 0, 1.f), wsr, lane * 16, kv);
                st16_dev(pack8(acc[tt][1], 8, 1.f), wsr, lane * 16, kv + 1024);
                st16_dev(pack8(acc[tt][2], 0, 1.f), wsr, lane * 16, kv + 2048);
                st16_dev(pack8(acc[tt][2], 8, 1.f), wsr, lane * 16, kv + 3072);
            }
            SBAR();
            float qs = QSCALE;
            asm volatile("s_nop 7\n\ts_nop 7" : "+v"(acc[0][0]), "+v"(acc[1][0]), "+v"(qs));
#pragma unroll
            for (int tt = 0; tt < 2; ++tt) {
                const unsigned q = WS_Q + (hd * 64 + 4 * mt + 2 * wm + tt) * 2048;
                st16_dev(pack8m(acc[tt][0], 0, qs), wsr, lane * 16, q);
                st16_dev(pack8m(acc[tt][0], 8, qs), wsr, lane * 16, q + 1024);
            }
            SBAR();
            WAIT_VM(0);
            if (lane == 0) flag_set(ws, FL_Q + hd * 128 + (mt * 2 + wm) * 4, epoch);
            __builtin_amdgcn_s_setprio(0);
            RAW_BAR(); SBAR();
        }
        const int TA = 8 * qt + 2 * wid;
        flag_wait(ws, FL_Q + head * 128, 32, 4, epoch, lane);
        __builtin_amdgcn_s_setprio(3);
        SBAR();
        bf16x8 k0 = BC8(ld16_dev(wsr, WS_KV + head * 262144 + lane * 16, 0));
        bf16x8 k1 = BC8(ld16_dev(wsr, WS_KV + head * 262144 + 1024 + lane * 16, 0));
        const bf16x8 qA0 = BC8(ld16_dev(wsr, WS_Q + (head * 64 + TA) * 2048 + lane * 16, 0));
        const bf16x8 qA1 = BC8(ld16_dev(wsr, WS_Q + (head * 64 + TA) * 2048 + 1024 + lane * 16, 0));
        const bf16x8 qB0 = BC8(ld16_dev(wsr, WS_Q + (head * 64 + TA) * 2048 + 2048 + lane * 16, 0));
        const bf16x8 qB1 = BC8(ld16_dev(wsr, WS_Q + (head * 64 + TA) * 2048 + 3072 + lane * 16, 0));
        bf16x8 v0 = BC8(ld16_dev(wsr, WS_KV + head * 262144 + 2048 + lane * 16, 0));
        u32x4 kv01[8];
#pragma unroll
        for (int i = 0; i < 8; ++i) kv01[i] = ld16_dev(wsr, WS_KV + head * 262144 + (i >> 2) * AT_STAGE + wid * 4096 + (i & 3) * 1024 + lane * 16, 0);
        l2_touch_raw(ws + WS_KV + (size_t)head * 262144, 262144, lane < 16 ? (2 + wid) * AT_STAGE + (qt * 16 + lane) * 128 : 0x40000000u, JUNK);
        SBAR();
        bf16x8 ones;
        { const int row = lane & 15, g = lane >> 4; const short v = (row == (g & 1)) ? (short)0x3f80 : (short)0;
#pragma unroll
          for (int i = 0; i < 8; ++i) ones[i] = v; }
        lds_ptr lp = lds + lane * 16;
        f32x16 oA = (f32x16){}, oB = (f32x16){};
        f32x4 lsA = (f32x4){}, lsB = (f32x4){};
        bf16x8 v1 = (bf16x8){};
        u32x4 pP = (u32x4){0u, 0u, 0u, 0u};
        asm volatile("" : "+{v[2:17]}"(oA), "+{v[18:33]}"(oB), "+{v[106:109]}"(lsA), "+{v[110:113]}"(lsB), "+{v[138:141]}"(v1), "+{v[142:145]}"(pP), "+{v[130:133]}"(ones));
        SBAR();
        float lA, lB;
        {
            f32x16 sA = MFMA32(k0, qA0, (f32x16){}); sA = MFMA32(k1, qA1, sA);
            f32x16 sB = MFMA32(k0, qB0, (f32x16){}); sB = MFMA32(k1, qB1, sB);
            float mx = sA[0], my = sB[0];
#pragma unroll
            for (int i = 1; i < 16; ++i) { mx = fmaxf(mx, sA[i]); my = fmaxf(my, sB[i]); }
            {
                auto rr = __builtin_amdgcn_permlane32_swap(__float_as_uint(mx), __float_as_uint(mx), false, false);
                mx = -fmaxf(__uint_as_float(rr[0]), __uint_as_float(rr[1]));
                auto rs = __builtin_amdgcn_permlane32_swap(__float_as_uint(my), __float_as_uint(my), false, false);
                my = -fmaxf(__uint_as_float(rs[0]), __uint_as_float(rs[1]));
            }
            SBAR();
#pragma unroll
            for (int i = 0; i < 8; ++i) *(LAS u32x4*)(lds + (i >> 2) * AT_STAGE + wid * 4096 + (i & 3) * 1024 + lane * 16) = kv01[i];
            WAIT_LGKM0();
            if (lane == 0) *(volatile LAS unsigned*)(lds + SYNC + wid * 4) = 1u;
            SBAR();
            f32x16 ngA, ngB;
#pragma unroll
            for (int i = 0; i < 16; ++i) { ngA[i] = mx; ngB[i] = my; sA[i] = add1(sA[i], mx); }
            asm volatile("" : "+{v[34:49]}"(ngB), "+{v[50:65]}"(ngA), "+{v[66:81]}"(sA));
            SBAR();

#define M32(d, a, b, c) "v_mfma_f32_32x32x16_bf16 " d ", " a ", " b ", " c "\n\t"
#define M16(d, b) "v_mfma_f32_16x16x32_bf16 " d ", v[130:133], " b ", " d "\n\t"
#define EX(i) "v_exp_f32_e32 v" #i ", v" #i "\n\t"
#define EX8(i) "v_exp_f32_e64 v" #i ", v" #i "\n\t"
#define CV(d, a, b) "v_cvt_pk_bf16_f32 v" #d ", v" #a ", v" #b "\n\t"
#define RD(d, base, off) "ds_read_b128 " d ", " base " offset:" #off "\n\t"
#define STEP_A(vc, vn, tb, V1OFF, nb, K0OFF, K1OFF, V0OFF)                                         \
            M32("v[82:97]", "v[102:105]", "v[114:117]", "v[34:49]") RD("v[102:105]", nb, K0OFF)    \
            EX(66) EX(67) EX(68) EX(69)                                                            \
            M32("v[82:97]", "v[98:101]", "v[118:121]", "v[82:97]") RD("v[98:101]", nb, K1OFF)      \
            EX(70) EX(71) CV(150, 66, 67) CV(151, 68, 69)                                          \
            M32("v[18:33]", "v[138:141]", "v[142:145]", "v[18:33]") RD("v[138:141]", tb, V1OFF)    \
            EX(72) EX(73) CV(152, 70, 71) CV(153, 72, 73)                                          \
            M16("v[110:113]", "v[142:145]") RD(vn, nb, V0OFF)                                      \
            EX(74) EX(75)                                                                          \
            M32("v[2:17]", vc, "v[150:153]", "v[2:17]")                                            \
            EX(76) EX(77) EX(78) EX(79)                                                            \
            M16("v[106:109]", "v[150:153]")                                                        \
            EX(80) EX(81) CV(142, 74, 75) CV(143, 76, 77) CV(144, 78, 79) CV(145, 80, 81)
#define STEP_B(vc)                                                                                 \
            "s_waitcnt lgkmcnt(0)\n\t" EX(82)                                                      \
            M32("v[66:81]", "v[102:105]", "v[122:125]", "v[50:65]")                                \
            EX8(83) EX(84) EX(85)                                                                  \
            M32("v[66:81]", "v[98:101]", "v[126:129]", "v[66:81]")                                 \
            EX(86) EX(87) CV(150, 82, 83) CV(151, 84, 85)                                          \
            M32("v[2:17]", "v[138:141]", "v[142:145]", "v[2:17]")                                  \
            EX(88) EX(89) CV(152, 86, 87) CV(153, 88, 89)                                          \
            M16("v[106:109]", "v[142:145]")                                                        \
            EX(90) EX(91)                                                                          \
            M32("v[18:33]", vc, "v[150:153]", "v[18:33]")                                          \
            EX(92) EX(93) EX(94) EX(95)                                                            \
            M16("v[110:113]", "v[150:153]")                                                        \
            EX(96) EX(97) CV(142, 90, 91) CV(143, 92, 93) CV(144, 94, 95) CV(145, 96, 97)
#define VA "v[134:137]"
#define VB "v[146:149]"
            {
                unsigned cnt = 16, off = AT_STAGE;
                const unsigned lpv = (unsigned)(size_t)lp;
                for (;;) {
                    u32x4 f;
                    asm volatile("ds_read_b128 %0, %1\n\ts_waitcnt lgkmcnt(0)" : "=v"(f) : "v"(SYNC) : "memory");
                    if (__builtin_amdgcn_readfirstlane(f[0] & f[1] & f[2] & f[3]) == 1) break;
                    __builtin_amdgcn_s_sleep(1);
                }
                __builtin_amdgcn_s_setprio(LOOP_PRIO);
                asm volatile(
                    "v_mov_b32_e32 v154, v156\n\t"
                    "v_add_u32_e32 v155, %[off], v156\n\t"
                    ".p2align 3\n"
                    "1:\n\t"
                    STEP_A(VA, VB, "v154", 3072, "v154", 4096, 5120, 6144) STEP_B(VA)
                    STEP_A(VB, VA, "v154", 7168, "v154", 8192, 9216, 10240) STEP_B(VB)
                    STEP_A(VA, VB, "v154", 11264, "v154", 12288, 13312, 14336) STEP_B(VA)
                    "s_barrier\n\t"
                    "s_addk_i32 %[off], 0x4000\n\t"
                    STEP_A(VB, VA, "v154", 15360, "v155", 0, 1024, 2048) STEP_B(VB)
                    "s_cmpk_eq_u32 %[off], 0xc000\n\t"
                    "s_cselect_b32 %[off], 0, %[off]\n\t"
                    "v_mov_b32_e32 v154, v155\n\t"
                    "v_add_u32_e32 v155, %[off], v156\n\t"
                    "s_addk_i32 %[cnt], -1\n\t"
                    "s_cmp_lg_u32 %[cnt], 0\n\t"
                    "s_cbranch_scc1 1b\n\t"
                    M32("v[18:33]", "v[138:141]", "v[142:145]", "v[18:33]")
                    M16("v[110:113]", "v[142:145]")
                    "s_nop 7\n\ts_nop 7"
                    : "+{v[2:17]}"(oA), "+{v[18:33]}"(oB), "+{v[66:81]}"(sA), "+{v[106:109]}"(lsA), "+{v[110:113]}"(lsB),
                      "+{v[102:105]}"(k0), "+{v[98:101]}"(k1), "+{v[134:137]}"(v0), "+{v[138:141]}"(v1), "+{v[142:145]}"(pP),
                      [cnt] "+s"(cnt), [off] "+s"(off)
                    : "{v[34:49]}"(ngB), "{v[50:65]}"(ngA), "{v[114:117]}"(qB0), "{v[118:121]}"(qB1), "{v[122:125]}"(qA0), "{v[126:129]}"(qA1),
                      "{v[130:133]}"(ones), "{v156}"(lpv)
                    : "v82", "v83", "v84", "v85", "v86", "v87", "v88", "v89", "v90", "v91", "v92", "v93", "v94", "v95", "v96", "v97",
                      "v146", "v147", "v148", "v149", "v150", "v151", "v152", "v153", "v154", "v155", "scc", "memory");
            }
            const float la = __shfl(lsA[0], r & 15), lb = __shfl(lsA[1], r & 15);
            lA = (r < 16) ? la : lb;
            const float lc = __shfl(lsB[0], r & 15), ld = __shfl(lsB[1], r & 15);
            lB = (r < 16) ? lc : ld;
        }
        {
            const char* kvg = ws + WS_KV + (size_t)head * 262144;
            bool bad = !(lA < 1.0e30f);
#pragma unroll
            for (int i = 0; i < 16; ++i) bad |= !(fabsf(oA[i]) < 1.0e37f);
            if (__any(bad)) attn_safe(kvg, qA0, qA1, lane, oA, lA);
            const unsigned og = WS_O + (TA * 32 + head) * 2048;
            const float rA = 1.0f / lA;
            st16_dev(pack8(oA, 0, rA), wsr, lane * 16, og);
            st16_dev(pack8(oA, 8, rA), wsr, lane * 16, og + 1024);
            bad = !(lB < 1.0e30f);
#pragma unroll
            for (int i = 0; i < 16; ++i) bad |= !(fabsf(oB[i]) < 1.0e37f);
            if (__any(bad)) attn_safe(kvg, qB0, qB1, lane, oB, lB);
            const float rB = 1.0f / lB;
            st16_dev(pack8(oB, 0, rB), wsr, lane * 16, og + 65536);
            st16_dev(pack8(oB, 8, rB), wsr, lane * 16, og + 65536 + 1024);
        }
        WAIT_VM(0);
        if (lane == 0) flag_set(ws, FL_O + (TA >> 2) * 256 + (head * 2 + (wid & 1)) * 4, epoch);
        RAW_BAR(); SBAR();
    }

    {
        const int tg2 = wid & 3, kh = wid >> 2;
        const int T2 = 4 * mt2 + tg2;
        flag_wait(ws, FL_O + mt2 * 256 + (32 * kh + ((T2 >> 1) & 1)) * 4, 16, 8, epoch, lane);
        SBAR();
        bf16x8 of[32];
        auto o_load = [&](int i) { of[i] = BC8(ld16_dev(wsr, lane * 16 + i * 1024, WS_O + T2 * 65536 + kh * 32768)); };
#pragma unroll
        for (int i = 0; i < G2_D; ++i) o_load(i);
        SBAR();
        const int wl = (4 * hh + ((lane >> 2) & 3)) * 64 + ((lane >> 4) & 1) * 32 + (lane & 3) * 8;
        lds_ptr wa = lds + WO_OFF + kh * 65536 + wl;
        lds_ptr wb = kh ? (lds + WO_TAIL + wl - 24 * 2048) : wa;
        f32x16 acc0 = (f32x16){}, acc1 = (f32x16){};
        s16x4 wf[2][4];
        auto w_load = [&](int kk) {
            lds_ptr b = (kk < 24 ? wa : wb) + kk * 2048;
            wf[kk & 1][0] = lds_tr(b); wf[kk & 1][1] = lds_tr(b + 1024); wf[kk & 1][2] = lds_tr(b + 512); wf[kk & 1][3] = lds_tr(b + 1024 + 512);
        };
        w_load(0);
        SBAR();
#pragma unroll
        for (int kk = 0; kk < 32; ++kk) {
            if (kk + 1 < 32) w_load(kk + 1);
            if (kk + G2_D < 32) o_load(kk + G2_D);
            SBAR();
            acc0 = MFMA32(cat4(wf[kk & 1][0], wf[kk & 1][1]), of[kk], acc0);
            acc1 = MFMA32(cat4(wf[kk & 1][2], wf[kk & 1][3]), of[kk], acc1);
            SBAR();
        }
        if (blockIdx.x == 0 && tid == 0) flag_set(ws, FL_EPOCH, epoch);
        lds_ptr ex = lds + kh * 32768 + tg2 * 4096 + lane * 16;
        const f32x16& snd = kh ? acc0 : acc1;
#pragma unroll
        for (int g = 0; g < 4; ++g) *(LAS f32x4*)(ex + g * 1024) = (f32x4){snd[4 * g], snd[4 * g + 1], snd[4 * g + 2], snd[4 * g + 3]};
        WAIT_LGKM0(); RAW_BAR(); SBAR();
        lds_ptr rx = lds + (kh ^ 1) * 32768 + tg2 * 4096 + lane * 16;
        f32x16 keep = kh ? acc1 : acc0;
#pragma unroll
        for (int g = 0; g < 4; ++g) {
            const f32x4 t = *(const LAS f32x4*)(rx + g * 1024);
#pragma unroll
            for (int i = 0; i < 4; ++i) keep[4 * g + i] += t[i];
        }
        const unsigned orow = (32 * T2 + r) * 4096 + 2048 + nt * 128 + kh * 64 + hh * 16;
        __builtin_amdgcn_raw_buffer_store_b128(rows8(keep, 0), outr, orow, 0, OUTB_AUX);
        __builtin_amdgcn_raw_buffer_store_b128(rows8(keep, 2), outr, orow + 32, 0, OUTB_AUX);
    }
}

extern "C" void kernel_launch(void* const* d_in, const int* in_sizes, int n_in, void* d_out, int out_size, void* d_ws, size_t ws_size,
                              hipStream_t stream) {
    Args a;
    a.aq = (const char*)d_in[0]; a.ak = (const char*)d_in[1]; a.av = (const char*)d_in[2];
    a.shk = (const char*)d_in[3]; a.ssmk = (const char*)d_in[4]; a.ssmd = (const char*)d_in[5];
    a.x = (const char*)d_in[6]; a.wqkv = (const char*)d_in[7]; a.wo = (const char*)d_in[8];
    a.out = (char*)d_out; a.ws = (char*)d_ws;
    (void)hipFuncSetAttribute((const void*)k_all, hipFuncAttributeMaxDynamicSharedMemorySize, ALL_LDS);
    k_all<<<256, 512, ALL_LDS, stream>>>(a);
}
```
